```python
import torch

def _softmax(x, axis=-1):
    return torch.softmax(x, dim=axis)

def _gelu(x):
    return torch.nn.functional.gelu(x, approximate='tanh')

def _silu(x):
    return torch.nn.functional.silu(x)

def _sigmoid(x):
    return torch.sigmoid(x)

def _layernorm(x, axis=-1, eps=1e-05):
    m = x.mean(dim=axis, keepdim=True)
    v = x.var(dim=axis, keepdim=True, unbiased=False)
    return (x - m) / torch.sqrt(v + eps)

def _scatter_add(tgt, idx, src, axis=0):
    return tgt.clone().index_add_(axis, idx, src)

def _segment_sum(src, seg, n):
    out = torch.zeros((n,) + src.shape[1:], dtype=src.dtype)
    return out.index_add_(0, seg, src)

def _pad(x, pw):
    flat = tuple((v for pair in reversed(pw) for v in pair))
    return torch.nn.functional.pad(x, flat)
kernel_name = 'parallel_sum__memory_aug_recurrent_mem_tr__glu_gated_reglu__prompt'

def setup_inputs():

    _w = lambda a: (lambda t: t.to(torch.bfloat16) if t.is_floating_point() else t)(torch.as_tensor(a))

    def _A_setup():
        B, S, Dm = (1, 2048, 512)
        H, Dh = (32, 128)
        M, seg = (8, 32)
        seg = min(seg, S)
        S = S // seg * seg
        return {'x': _w((lambda *s: torch.randn(*s, dtype=torch.bfloat16) * 0.05)(B, S, Dm)), 'mem0': _w((lambda *s: torch.randn(*s, dtype=torch.bfloat16) * 0.2)(B, M, Dm)), 'Wq': _w((lambda *s: torch.randn(*s, dtype=torch.bfloat16) * 1.6)(Dm, H, Dh) * Dm ** (-0.5)), 'Wk': _w((lambda *s: torch.randn(*s, dtype=torch.bfloat16) * 1.6)(Dm, H, Dh) * Dm ** (-0.5)), 'Wv': _w((lambda *s: torch.randn(*s, dtype=torch.bfloat16) * 0.2)(Dm, H, Dh) * Dm ** (-0.5)), 'Wo': _w((lambda *s: torch.randn(*s, dtype=torch.bfloat16) * 0.2)(H, Dh, Dm) * (H * Dh) ** (-0.5)), 'seg_len': seg}
    _A_in = _A_setup()

    def _B_setup():
        B, S, D, F = (1, 2048, 512, 1792)
        return {'x': _w((lambda *s: torch.randn(*s, dtype=torch.bfloat16) * 0.05)(B, S, D)), 'W_gate': _w((lambda *s: torch.randn(*s, dtype=torch.bfloat16) * 0.2)(D, F) * (2.0 / D) ** 0.5), 'W_up': _w((lambda *s: torch.randn(*s, dtype=torch.bfloat16) * 0.2)(D, F) * (2.0 / D) ** 0.5), 'W_down': _w((lambda *s: torch.randn(*s, dtype=torch.bfloat16) * 0.2)(F, D) * (1.0 / F) ** 0.5)}
    _B_in = _B_setup()
    return {'A_x': _A_in['x'], 'A_mem0': _A_in['mem0'], 'A_Wq': _A_in['Wq'], 'A_Wk': _A_in['Wk'], 'A_Wv': _A_in['Wv'], 'A_Wo': _A_in['Wo'], 'A_seg_len': _A_in['seg_len'], 'B_x': _B_in['x'], 'B_W_gate': _B_in['W_gate'], 'B_W_up': _B_in['W_up'], 'B_W_down': _B_in['W_down']}

def reference(A_x, A_mem0, A_Wq, A_Wk, A_Wv, A_Wo, A_seg_len, B_x, B_W_gate, B_W_up, B_W_down):

    def _A_ref(x, mem0, Wq, Wk, Wv, Wo, seg_len):
        B, S, Dm = x.shape
        H, Dh = (Wq.shape[1], Wq.shape[2])
        M = mem0.shape[1]
        scale = Dh ** (-0.5)
        n_seg = S // seg_len
        L = M + seg_len
        idx = torch.arange(L)
        mask = (idx[:, None] < M) | (idx[:, None] >= idx[None, :])
        mem = mem0
        outs = []
        for s in range(n_seg):
            xs = x[:, s * seg_len:(s + 1) * seg_len, :].clone()
            h = torch.concatenate([mem, xs], axis=1)
            q = torch.einsum('btd,dhe->bhte', h, Wq)
            k = torch.einsum('btd,dhe->bhte', h, Wk)
            v = torch.einsum('btd,dhe->bhte', h, Wv)
            scores = torch.einsum('bhqe,bhke->bhqk', q, k) * scale
            scores = torch.where(mask[None, None], scores, -1000000000.0)
            p = _softmax(scores, axis=-1)
            o = torch.einsum('bhqk,bhke->bhqe', p, v)
            o = torch.einsum('bhte,hed->btd', o, Wo)
            mem = o[:, :M, :].clone()
            outs.append(o[:, M:, :])
        return torch.concatenate(outs, axis=1)

    def _B_ref(x, W_gate, W_up, W_down):
        gate = torch.einsum('bsd,df->bsf', x, W_gate)
        up = torch.einsum('bsd,df->bsf', x, W_up)
        gate = gate * (lambda x, dt: x.to(dt))(gate > 0, gate.dtype)
        h = gate * up
        return torch.einsum('bsf,fd->bsd', h, W_down)
    x = A_x
    xn = _layernorm(x, axis=-1)
    return x + _A_ref(xn, A_mem0, A_Wq, A_Wk, A_Wv, A_Wo, A_seg_len) + _B_ref(xn, B_W_gate, B_W_up, B_W_down)
```

```cpp
#include <hip/hip_runtime.h>
#include <stdint.h>
namespace refimpl {
typedef unsigned short bf;
__device__ __forceinline__ float b2f(bf v) { return __uint_as_float(((unsigned)v) << 16); }
__device__ __forceinline__ bf f2b(float f) { unsigned u = __float_as_uint(f); if ((u & 0x7fffffffu) > 0x7f800000u) return (bf)((u >> 16) | 0x40); return (bf)((u + 0x7fffu + ((u >> 16) & 1)) >> 16); }
__device__ __forceinline__ float rb(float f) { return b2f(f2b(f)); }

__global__ void k_ln(const bf* x, bf* xn) {
  __shared__ float red[256];
  const int row = blockIdx.x, t = threadIdx.x;
  float a = b2f(x[row * 512 + t]), b = b2f(x[row * 512 + 256 + t]);
  red[t] = a + b; __syncthreads();
  for (int s = 128; s > 0; s >>= 1) { if (t < s) red[t] += red[t + s]; __syncthreads(); }
  const float mean = red[0] / 512.f; __syncthreads();
  red[t] = (a - mean) * (a - mean) + (b - mean) * (b - mean); __syncthreads();
  for (int s = 128; s > 0; s >>= 1) { if (t < s) red[t] += red[t + s]; __syncthreads(); }
  const float var = red[0] / 512.f;
  const float m = rb(mean), v = rb(var), den = rb(sqrtf(rb(v + 1e-5f)));
  xn[row * 512 + t] = f2b(rb(a - m) / den);
  xn[row * 512 + 256 + t] = f2b(rb(b - m) / den);
}
__global__ void __launch_bounds__(256) k_gemm(const bf* A, int lda, const bf* B, int ldb, bf* C, int ldc, int M, int N, int K) {
  __shared__ float sA[16][65], sB[16][65];
  const int bm = blockIdx.y * 64, bn = blockIdx.x * 64, t = threadIdx.x, tx = t & 15, ty = t >> 4;
  float acc[4][4] = {};
  for (int k0 = 0; k0 < K; k0 += 16) {
    for (int e = t; e < 1024; e += 256) { int r = e >> 4, c = e & 15; sA[c][r] = (bm + r < M) ? b2f(A[(size_t)(bm + r) * lda + k0 + c]) : 0.f; }
    for (int e = t; e < 1024; e += 256) { int r = e >> 6, c = e & 63; sB[r][c] = (bn + c < N) ? b2f(B[(size_t)(k0 + r) * ldb + bn + c]) : 0.f; }
    __syncthreads();
#pragma unroll
    for (int k = 0; k < 16; k++) {
      float a[4], b[4];
#pragma unroll
      for (int i = 0; i < 4; i++) { a[i] = sA[k][ty * 4 + i]; b[i] = sB[k][tx * 4 + i]; }
#pragma unroll
      for (int i = 0; i < 4; i++)
#pragma unroll
        for (int j = 0; j < 4; j++) acc[i][j] = fmaf(a[i], b[j], acc[i][j]);
    }
    __syncthreads();
  }
  for (int i = 0; i < 4; i++) for (int j = 0; j < 4; j++) { int r = bm + ty * 4 + i, c = bn + tx * 4 + j; if (r < M && c < N) C[(size_t)r * ldc + c] = f2b(acc[i][j]); }
}
__global__ void __launch_bounds__(256) k_attn(const bf* qm, const bf* km, const bf* vm, const bf* Q, const bf* K, const bf* V, int s, bf* o  ) {
  __shared__ float sq[40][128], sk[40][129], sv[40][128], sp[40][41];
  const int h = blockIdx.x, t = threadIdx.x;
  for (int e = t; e < 40 * 128; e += 256) {
    int r = e >> 7, c = e & 127;
    size_t off = r < 8 ? (size_t)r * 4096 + h * 128 + c : (size_t)(s * 32 + r - 8) * 4096 + h * 128 + c;
    sq[r][c] = b2f(r < 8 ? qm[off] : Q[off]); sk[r][c] = b2f(r < 8 ? km[off] : K[off]); sv[r][c] = b2f(r < 8 ? vm[off] : V[off]);
  }
  __syncthreads();
  const float scale = 0.08838834764831845f;
  for (int e = t; e < 1600; e += 256) {
    int i = e / 40, j = e % 40; float a = 0;
    for (int c = 0; c < 128; c++) a = fmaf(sq[i][c], sk[j][c], a);
    a = rb(rb(a) * scale);
    if (!(i < 8 || i >= j)) a = rb(-1e9f);
    sp[i][j] = a;
  }
  __syncthreads();
  if (t < 40) {
    float m = -INFINITY; for (int j = 0; j < 40; j++) m = fmaxf(m, sp[t][j]);
    float sum = 0; for (int j = 0; j < 40; j++) { float e = expf(sp[t][j] - m); sp[t][j] = e; sum += e; }
    for (int j = 0; j < 40; j++) sp[t][j] = rb(sp[t][j] / sum);
  }
  __syncthreads();
  for (int e = t; e < 40 * 128; e += 256) {
    int i = e >> 7, c = e & 127; float a = 0;
    for (int j = 0; j < 40; j++) a = fmaf(sp[i][j], sv[j][c], a);
    o[(size_t)i * 4096 + h * 128 + c] = f2b(a);
  }
}
__global__ void k_reglu(const bf* g, const bf* u, bf* h, int n) { int i = blockIdx.x * 256 + threadIdx.x; if (i < n) { float a = b2f(g[i]); a = rb(a * (a > 0.f ? 1.f : 0.f)); h[i] = f2b(a * b2f(u[i])); } }
__global__ void k_final(const bf* x, const bf* a, const bf* b, bf* out, int n) { int i = blockIdx.x * 256 + threadIdx.x; if (i < n) out[i] = f2b(rb(b2f(x[i]) + b2f(a[i])) + b2f(b[i])); }
__global__ void k_copy(const bf* src, bf* dst, int n) { int i = blockIdx.x * 256 + threadIdx.x; if (i < n) dst[i] = src[i]; }

struct Bufs { bf *xn, *Q, *K, *V, *mem  , *qm, *km, *vm  , *o  , *oproj  , *A, *g, *u, *h, *B; };
inline size_t ws_bytes() { return (size_t)2 * (2048 * 512 + 3 * 2048 * 4096 + 65 * 8 * 512 + 3 * 64 * 8 * 4096 + 64 * 40 * 4096 + 64 * 40 * 512 + 2048 * 512 + 3 * 2048 * 1792 + 2048 * 512) + 4096; }
inline Bufs carve(void* ws) {
  Bufs b; bf* p = (bf*)ws;
  b.xn = p; p += 2048 * 512; b.Q = p; p += 2048 * 4096; b.K = p; p += 2048 * 4096; b.V = p; p += 2048 * 4096;
  b.mem = p; p += 65 * 8 * 512; b.qm = p; p += 64 * 8 * 4096; b.km = p; p += 64 * 8 * 4096; b.vm = p; p += 64 * 8 * 4096;
  b.o = p; p += 64 * 40 * 4096; b.oproj = p; p += 64 * 40 * 512; b.A = p; p += 2048 * 512;
  b.g = p; p += 2048 * 1792; b.u = p; p += 2048 * 1792; b.h = p; p += 2048 * 1792; b.B = p; p += 2048 * 512;
  return b;
}
inline void run(const bf* x, const bf* mem0, const bf* Wq, const bf* Wk, const bf* Wv, const bf* Wo, const bf* Wg, const bf* Wu, const bf* Wd, bf* out, void* ws, hipStream_t st) {
  Bufs b = carve(ws);
  k_ln<<<2048, 256, 0, st>>>(x, b.xn);
  k_gemm<<<dim3(64, 32), 256, 0, st>>>(b.xn, 512, Wq, 4096, b.Q, 4096, 2048, 4096, 512);
  k_gemm<<<dim3(64, 32), 256, 0, st>>>(b.xn, 512, Wk, 4096, b.K, 4096, 2048, 4096, 512);
  k_gemm<<<dim3(64, 32), 256, 0, st>>>(b.xn, 512, Wv, 4096, b.V, 4096, 2048, 4096, 512);
  k_gemm<<<dim3(28, 32), 256, 0, st>>>(b.xn, 512, Wg, 1792, b.g, 1792, 2048, 1792, 512);
  k_gemm<<<dim3(28, 32), 256, 0, st>>>(b.xn, 512, Wu, 1792, b.u, 1792, 2048, 1792, 512);
  k_reglu<<<2048 * 1792 / 256, 256, 0, st>>>(b.g, b.u, b.h, 2048 * 1792);
  k_gemm<<<dim3(8, 32), 256, 0, st>>>(b.h, 1792, Wd, 512, b.B, 512, 2048, 512, 1792);
  k_copy<<<16, 256, 0, st>>>(mem0, b.mem, 4096);
  for (int s = 0; s < 64; s++) {
    bf* m = b.mem + s * 4096;
    bf *qm = b.qm + (size_t)s * 8 * 4096, *km = b.km + (size_t)s * 8 * 4096, *vm = b.vm + (size_t)s * 8 * 4096;
    k_gemm<<<dim3(64, 1), 256, 0, st>>>(m, 512, Wq, 4096, qm, 4096, 8, 4096, 512);
    k_gemm<<<dim3(64, 1), 256, 0, st>>>(m, 512, Wk, 4096, km, 4096, 8, 4096, 512);
    k_gemm<<<dim3(64, 1), 256, 0, st>>>(m, 512, Wv, 4096, vm, 4096, 8, 4096, 512);
    bf* o = b.o + (size_t)s * 40 * 4096; bf* op = b.oproj + (size_t)s * 40 * 512;
    k_attn<<<32, 256, 0, st>>>(qm, km, vm, b.Q, b.K, b.V, s, o);
    k_gemm<<<dim3(8, 1), 256, 0, st>>>(o, 4096, Wo, 512, op, 512, 40, 512, 4096);
    k_copy<<<16, 256, 0, st>>>(op, b.mem + (s + 1) * 4096, 4096);
    k_copy<<<64, 256, 0, st>>>(op + 8 * 512, b.A + (size_t)s * 32 * 512, 32 * 512);
  }
  k_final<<<2048 * 512 / 256, 256, 0, st>>>(x, b.A, b.B, out, 2048 * 512);
}
}

#include <hip/hip_bf16.h>
extern "C" void kernel_launch(void* const* d_in, const int* in_sizes, int n_in, void* d_out, int out_size, void* d_ws, size_t ws_size, hipStream_t stream) {
  using refimpl::bf;
  refimpl::run((const bf*)d_in[0], (const bf*)d_in[1], (const bf*)d_in[2], (const bf*)d_in[3], (const bf*)d_in[4], (const bf*)d_in[5],
               (const bf*)d_in[8], (const bf*)d_in[9], (const bf*)d_in[10], (bf*)d_out, d_ws, stream);
}
```

```cpp
#include <hip/hip_runtime.h>
#include <stdint.h>
#include <type_traits>

namespace {
typedef unsigned short bf16_t;
typedef __bf16 bf8 __attribute__((ext_vector_type(8)));
typedef __bf16 bf4 __attribute__((ext_vector_type(4)));
typedef short s4v __attribute__((ext_vector_type(4)));
typedef float f4 __attribute__((ext_vector_type(4)));
typedef unsigned u4 __attribute__((ext_vector_type(4)));
typedef unsigned u2 __attribute__((ext_vector_type(2)));
#define LAS __attribute__((address_space(3)))
#define GAS __attribute__((address_space(1)))

constexpr unsigned OFF_CTL = 0u;
constexpr unsigned OFF_PRING = 1048576u;
constexpr unsigned OFF_FLG = 2097152u;
constexpr unsigned OFF_XN = 3145728u;
constexpr unsigned OFF_Q = 8388608u;
constexpr unsigned OFF_K = 25165824u;
constexpr unsigned OFF_VT = 41943040u;
constexpr unsigned OFF_H = 58720256u;
constexpr unsigned OFF_KVM = 67108864u;
constexpr unsigned OFF_PART = 75497472u;
constexpr unsigned OFF_DBG = 213909504u;
constexpr unsigned WS_USED = 214958080u;
constexpr unsigned FLG_G1 = 0u;
constexpr unsigned FLG_T = 64u;
constexpr unsigned FLG_KV = 128u;
constexpr unsigned FLG_SEL = 160u;
constexpr unsigned FLG_TMO = 161u;
constexpr unsigned FLG_EPOCH = 162u;
constexpr unsigned INIT_G1 = 192u;
constexpr unsigned INIT_T = 256u;
constexpr unsigned INIT_H = 320u;
constexpr unsigned INIT_R = 352u;
constexpr unsigned FLG_LN = 416u;

constexpr unsigned flg(unsigned i) { return OFF_FLG + i * 128u; }
constexpr int kSpin = 400000;
#define HX0 2
#define HX1 3
#define RX0 0

#define NPROBE 8
#define CALW 1
#define CALJ 63
#define START_DELAY 6
constexpr unsigned kLds = 160 * 1024;

struct Params {
  const bf16_t *x, *mem0, *wq, *wk, *wv, *wo, *wg, *wu, *wd;
  bf16_t* out;
  char* ws;
};

__device__ __forceinline__ float b2f(unsigned v) { return __uint_as_float(v << 16); }
__device__ __forceinline__ unsigned pk2(float lo, float hi) {
  typedef __bf16 b2 __attribute__((ext_vector_type(2)));
  typedef float f2 __attribute__((ext_vector_type(2)));
  f2 v = {lo, hi};
  return __builtin_bit_cast(unsigned, __builtin_convertvector(v, b2));
}
__device__ __forceinline__ float rbf(float f) { return b2f(pk2(f, 0.f) & 0xffffu); }
__device__ __forceinline__ f4 mfma32(u4 a, u4 b, f4 c) { return __builtin_amdgcn_mfma_f32_16x16x32_bf16(__builtin_bit_cast(bf8, a), __builtin_bit_cast(bf8, b), c, 0, 0, 0); }
__device__ __forceinline__ f4 mfma16(u2 a, u2 b, f4 c) { return __builtin_amdgcn_mfma_f32_16x16x16bf16_1k(__builtin_bit_cast(s4v, a), __builtin_bit_cast(s4v, b), c, 0, 0, 0); }
template <int IMM> __device__ __forceinline__ void glds16(const void* gsrc, unsigned base) {
  unsigned keep;
  asm volatile("s_mov_b32 %0, m0\n\ts_add_u32 m0, %2, %3\n\ts_nop 0\n\tglobal_load_lds_dwordx4 %1, off\n\ts_mov_b32 m0, %0" : "=&s"(keep) : "v"(gsrc), "s"(base), "i"(IMM) : "memory", "scc");
}
template <int IMM> __device__ __forceinline__ void glds16_sc1(const void* gsrc, unsigned base) {
  unsigned keep;
  asm volatile("s_mov_b32 %0, m0\n\ts_add_u32 m0, %2, %3\n\ts_nop 0\n\tglobal_load_lds_dwordx4 %1, off sc1\n\ts_mov_b32 m0, %0" : "=&s"(keep) : "v"(gsrc), "s"(base), "i"(IMM) : "memory", "scc");
}
template <int N, class F> __device__ __forceinline__ void static_for(F&& f) {
  if constexpr (N > 0) { static_for<N - 1>(f); f(std::integral_constant<int, N - 1>{}); }
}
__device__ __forceinline__ float wave_sum(float v) {
  v += __builtin_bit_cast(float, __builtin_amdgcn_update_dpp(0, __builtin_bit_cast(int, v), 0xB1, 0xf, 0xf, true));
  v += __builtin_bit_cast(float, __builtin_amdgcn_update_dpp(0, __builtin_bit_cast(int, v), 0x4E, 0xf, 0xf, true));
  v += __builtin_bit_cast(float, __builtin_amdgcn_update_dpp(0, __builtin_bit_cast(int, v), 0x141, 0xf, 0xf, true));
  v += __builtin_bit_cast(float, __builtin_amdgcn_update_dpp(0, __builtin_bit_cast(int, v), 0x140, 0xf, 0xf, true));
  { const auto r = __builtin_amdgcn_permlane16_swap(__float_as_uint(v), __float_as_uint(v), false, false); v = __uint_as_float(r[0]) + __uint_as_float(r[1]); }
  { const auto r = __builtin_amdgcn_permlane32_swap(__float_as_uint(v), __float_as_uint(v), false, false); v = __uint_as_float(r[0]) + __uint_as_float(r[1]); }
  return v;
}
#define WAIT_VM(n) asm volatile("s_waitcnt vmcnt(" #n ")" ::: "memory")
#define WAIT_LGKM0() asm volatile("s_waitcnt lgkmcnt(0)" ::: "memory")
__device__ __forceinline__ void barrier() { asm volatile("s_waitcnt lgkmcnt(0)\n\ts_barrier" ::: "memory"); }
__device__ __forceinline__ u4 lds_rd128(unsigned addr) { return *(const LAS u4*)(uintptr_t)addr; }
__device__ __forceinline__ u2 lds_rd64(unsigned addr) { return *(const LAS u2*)(uintptr_t)addr; }
__device__ __forceinline__ u2 lds_tr(unsigned addr) { return __builtin_bit_cast(u2, __builtin_amdgcn_ds_read_tr16_b64_v4i16((LAS s4v*)(uintptr_t)addr)); }
__device__ __forceinline__ int ld_flag(char* ws, unsigned off) { return (int)__hip_atomic_load((GAS unsigned*)(ws + off), __ATOMIC_RELAXED, __HIP_MEMORY_SCOPE_AGENT); }
__device__ __forceinline__ void st_flag(char* ws, unsigned off, int v) { __hip_atomic_store((GAS unsigned*)(ws + off), (unsigned)v, __ATOMIC_RELAXED, __HIP_MEMORY_SCOPE_AGENT); }
__device__ __forceinline__ void st_tag(char* ws, unsigned off, unsigned epoch, unsigned payload) {
  __hip_atomic_store((GAS unsigned long long*)(ws + off), ((unsigned long long)epoch << 32) | payload, __ATOMIC_RELAXED, __HIP_MEMORY_SCOPE_AGENT);
}
__device__ __forceinline__ bool wait_tags(char* ws, unsigned off, bool mine, unsigned epoch, unsigned* payload = nullptr) {
  for (int spin = 0; spin < kSpin; spin++) {
    const unsigned long long v = mine ? __hip_atomic_load((GAS unsigned long long*)(ws + off), __ATOMIC_RELAXED, __HIP_MEMORY_SCOPE_AGENT) : (unsigned long long)epoch << 32;
    if (__all((unsigned)(v >> 32) == epoch)) { if (payload) *payload = (unsigned)v; return true; }
    __builtin_amdgcn_s_sleep(2);
  }
  st_flag(ws, flg(FLG_TMO), 5);
  return false;
}
__device__ __forceinline__ bool wait_flags(char* ws, unsigned off, bool mine, int need) {
  for (int spin = 0; spin < kSpin; spin++) {
    const int v = mine ? ld_flag(ws, off) : need;
    if (__all(v >= need)) return true;
    __builtin_amdgcn_s_sleep(2);
  }
  st_flag(ws, flg(FLG_TMO), 3);
  return false;
}

__device__ __forceinline__ void dstamp(const Params&, int, int, int) {}
__device__ __forceinline__ void cstamp(const Params&, int, int, int) {}
template <int TYPE>
__device__ __forceinline__ void g1_role(const Params& p, int m, const bf16_t* W0, const bf16_t* W1, int ldw, unsigned outOff, unsigned flagOff) {
  const int tid = threadIdx.x, w = __builtin_amdgcn_readfirstlane(tid >> 6), l = tid & 63, c = l & 15, g = l >> 4;
  const bf16_t* xn = (const bf16_t*)(p.ws + OFF_XN);
  constexpr bool PERM = TYPE == 1 || TYPE == 3;
  auto key = [&](int row) { return PERM ? (((row >> 3) << 2) | (row & 3)) : (row & 15); };
  auto xbuf = [&](int s) { const int k = s % 3; return (unsigned)(k == 0 ? 131072 : (k - 1) * 32768); };
  auto issueX = [&](int s) {
    const unsigned dst = xbuf(s) + w * 8192;
    static_for<8>([&](auto I) {
      constexpr int i = I.value;
      const int row = w * 8 + i;
      glds16_sc1<i * 1024>(xn + (size_t)(s * 32 + row) * 512 + (l ^ key(row)) * 8, dst);
    });
  };
  const unsigned epoch = (unsigned)ld_flag(p.ws, flg(FLG_EPOCH)) + 1;
  if (tid == 0) st_flag(p.ws, flagOff, -1);
  if (TYPE == 0 || TYPE == 2) { for (int i = 0; i < START_DELAY; i++) __builtin_amdgcn_s_sleep(35); }
  u4 Wf[16][4];
  {
    auto issue = [&](int ch) {
      const unsigned dst = (ch & 1) * 65536 + w * 16384;
      static_for<16>([&](auto I) {
        constexpr int i = I.value;
        const int row = 2 * (w * 16 + i) + (l >> 5), gran = (l & 31) ^ ((row & 3) << 2);
        const bf16_t* src = TYPE == 2 ? ((gran < 16 ? W0 : W1) + (size_t)(ch * 128 + row) * ldw + 128 * m + (gran & 15) * 8)
                                      : (W0 + (size_t)(ch * 128 + row) * ldw + 256 * m + gran * 8);
        glds16<i * 1024>(src, dst);
      });
    };
    issue(0); issue(1);
    const int q = c >> 2, pp = c & 3;
#pragma unroll
    for (int ch = 0; ch < 4; ch++) {
      if (ch == 2) {
        if (!wait_tags(p.ws, flg(FLG_LN + (l >> 3)) + (l & 3) * 8, true, epoch)) return;
        issueX(0);
      }
      if (ch < 2) WAIT_VM(16); else if (ch == 2) WAIT_VM(24); else WAIT_VM(8);
      barrier();
      if (ch == 0 && tid == 0) st_tag(p.ws, flagOff - flg(FLG_G1) + flg(INIT_G1), epoch, 1);
#pragma unroll
      for (int ksl = 0; ksl < 4; ksl++)
#pragma unroll
        for (int t = 0; t < 4; t++) {
          const int colb = (TYPE == 0 || TYPE == 3) ? 128 * w + 64 * (t >> 1) + 16 * pp + 8 * (t & 1)
                         : TYPE == 1 ? 128 * w + 32 * t + 8 * pp
                                     : (t >> 1) * 256 + 64 * w + 16 * pp + 8 * (t & 1);
          u2 h[2];
#pragma unroll
          for (int n = 0; n < 2; n++) {
            const int row = 32 * ksl + 8 * g + 4 * n + q;
            h[n] = lds_tr((ch & 1) * 65536 + row * 512 + (((colb >> 4) ^ (q << 2)) << 4) + (colb & 8));
          }
          Wf[ch * 4 + ksl][t] = u4{h[0][0], h[0][1], h[1][0], h[1][1]};
        }
      barrier();
      if (ch + 2 < 4) issue(ch + 2);
    }
  }
  const auto rs = __builtin_amdgcn_make_buffer_rsrc(p.ws, 0, WS_USED, 0x00020000);
  issueX(1);
  for (int s = 0; s < 64; s++) {
    if (s == 0) WAIT_VM(8);
    barrier();
    if (s == 5) {
      for (int spin = 0;; spin++) {
        const GAS u4* a = (const GAS u4*)(p.ws + flg(FLG_LN + l));
        u4 x0, x1;
        asm volatile("global_load_dwordx4 %0, %2, off sc1\n\tglobal_load_dwordx4 %1, %2, off offset:16 sc1\n\ts_waitcnt vmcnt(0)" : "=&v"(x0), "=&v"(x1) : "v"(a) : "memory");
        if (__all(x0[1] == epoch && x0[3] == epoch && x1[1] == epoch && x1[3] == epoch)) break;
        if (spin > kSpin) { st_flag(p.ws, flg(FLG_TMO), 6); return; }
      }
    }
    if (s + 2 < 64) issueX(s + 2);
    f4 acc[2][4];
    {
      const unsigned base0 = xbuf(s) + (PERM ? (8 * (c >> 2) + (c & 3)) : c) * 1024;
      constexpr unsigned tstride = PERM ? 4096 : 16384;
      u4 xa[16], xb[16];
#pragma unroll
      for (int ks = 0; ks < 16; ks++) xa[ks] = lds_rd128(base0 + (((4 * ks + g) ^ c) << 4));
      __builtin_amdgcn_sched_barrier(0);
#pragma unroll
      for (int t = 0; t < 4; t++) { acc[0][t] = f4{0, 0, 0, 0}; acc[1][t] = f4{0, 0, 0, 0}; }
#pragma unroll
      for (int ks = 0; ks < 16; ks++) {
        xb[ks] = lds_rd128(base0 + tstride + (((4 * ks + g) ^ c) << 4));
#pragma unroll
        for (int t = 0; t < 4; t++) acc[0][t] = TYPE == 1 ? mfma32(xa[ks], Wf[ks][t], acc[0][t]) : mfma32(Wf[ks][t], xa[ks], acc[0][t]);
        __builtin_amdgcn_sched_barrier(0);
      }
#pragma unroll
      for (int ks = 0; ks < 16; ks++)
#pragma unroll
        for (int t = 0; t < 4; t++) acc[1][t] = TYPE == 1 ? mfma32(xb[ks], Wf[ks][t], acc[1][t]) : mfma32(Wf[ks][t], xb[ks], acc[1][t]);
    }
    if (s + 2 < 64) WAIT_VM(8); else WAIT_VM(0);
    if (s >= 4) { barrier(); if (tid == 0) st_flag(p.ws, flagOff, s - 1); }
#pragma unroll
    for (int tt = 0; tt < 2; tt++) {
      if (TYPE == 0 || TYPE == 3) {
        const int h = 2 * m + (w >> 1);
#pragma unroll
        for (int P = 0; P < 2; P++) {
          const f4 a = acc[tt][2 * P], b = acc[tt][2 * P + 1];
          const u4 v = {pk2(a[0], a[1]), pk2(a[2], a[3]), pk2(b[0], b[1]), pk2(b[2], b[3])};
          __builtin_amdgcn_raw_buffer_store_b128(v, rs, outOff + (s * 32 + h) * 8192 + (tt * 4 + 2 * (w & 1) + P) * 1024 + l * 16, 0, 16);
        }
      } else if (TYPE == 2) {
        const int fb = 2 * m + (w >> 1);
        unsigned o[4];
#pragma unroll
        for (int e = 0; e < 2; e++)
#pragma unroll
          for (int r = 0; r < 4; r += 2) {
            float hv[2];
#pragma unroll
            for (int k = 0; k < 2; k++) {
              const float gt = rbf(acc[tt][e][r + k]), up = rbf(acc[tt][2 + e][r + k]);
              hv[k] = (gt > 0.f ? gt : gt * 0.f) * up;
            }
            o[e * 2 + (r >> 1)] = pk2(hv[0], hv[1]);
          }
        __builtin_amdgcn_raw_buffer_store_b128(u4{o[0], o[1], o[2], o[3]}, rs, outOff + (s * 28 + fb) * 4096 + (tt * 2 + (w & 1)) * 1024 + l * 16, 0, 16);
      }
    }
    if (TYPE == 1) {
      const int h = 2 * m + (w >> 1);
#pragma unroll
      for (int t = 0; t < 4; t++) {
        const f4 a = acc[0][t], b = acc[1][t];
        const u4 v = {pk2(a[0], a[1]), pk2(a[2], a[3]), pk2(b[0], b[1]), pk2(b[2], b[3])};
        __builtin_amdgcn_raw_buffer_store_b128(v, rs, outOff + (s * 32 + h) * 8192 + (64 * (w & 1) + 16 * t + c) * 64 + g * 16, 0, 16);
      }
    }
    if (s < 3) {
      WAIT_VM(0); barrier();
      if (tid == 0) st_flag(p.ws, flagOff, s);
    }
  }
  WAIT_VM(0); barrier();
  if (tid == 0) st_flag(p.ws, flagOff, 63);
}

__device__ __forceinline__ unsigned part_off(unsigned lin, unsigned farsel) {
  return OFF_PART + ((lin >> 13) << 14) + (lin & 8191u) + (((farsel ^ (lin >> 20)) & 1u) << 13);
}
__device__ __forceinline__ int pslot(int s) {
  return s & 3;
}
__device__ __forceinline__ bool reduce_tile(const Params& p, int r, int s, unsigned redOff, unsigned farsel) {
  const int tid = threadIdx.x, w = tid >> 6, l = tid & 63;
  const int tt = r >> 4, col0 = 128 * (r & 3) + 32 * ((r >> 2) & 3);
  const auto rs = __builtin_amdgcn_make_buffer_rsrc(p.ws, 0, WS_USED, 0x00020000);
  LAS float* red = (LAS float*)(uintptr_t)redOff;
  if (!wait_flags(p.ws, flg(FLG_T + (s & 1) * 32 + (l & 31)), l < 32, s)) return false;
  u4 v[8];
#pragma unroll
  for (int i = 0; i < 8; i++) v[i] = __builtin_amdgcn_raw_buffer_load_b128(rs, part_off((unsigned)(pslot(s) * 32 + w * 8 + i) * 32768u + r * 1024, farsel) + l * 16, 0, 16);
  const int fl = tid >> 2, k = tid & 3;
  const size_t oidx = (size_t)(s * 32 + 16 * tt + (fl & 15)) * 512 + col0 + 8 * (fl >> 4) + 2 * k;
  const unsigned xv = *(const unsigned*)(p.x + oidx);
  float a[8] = {0, 0, 0, 0, 0, 0, 0, 0};
#pragma unroll
  for (int i = 0; i < 8; i++)
#pragma unroll
    for (int q = 0; q < 4; q++) { a[2 * q] += b2f(v[i][q] & 0xffffu); a[2 * q + 1] += __uint_as_float(v[i][q] & 0xffff0000u); }
  *(LAS f4*)(red + w * 512 + l * 8) = f4{a[0], a[1], a[2], a[3]};
  *(LAS f4*)(red + w * 512 + l * 8 + 4) = f4{a[4], a[5], a[6], a[7]};
  barrier();
  float o0 = b2f(xv & 0xffffu), o1 = __uint_as_float(xv & 0xffff0000u);
#pragma unroll
  for (int q = 0; q < 4; q++) { o0 += red[q * 512 + tid * 2]; o1 += red[q * 512 + tid * 2 + 1]; }
  *(unsigned*)(p.out + oidx) = pk2(o0, o1);
  return true;
}

__device__ __forceinline__ void t_role(const Params& p, int h, int par) {
  const int tid = threadIdx.x, w = __builtin_amdgcn_readfirstlane(tid >> 6), l = tid & 63, c = l & 15, g = l >> 4;
  const bool ffn = h < 28;
  const unsigned epoch = (unsigned)ld_flag(p.ws, flg(FLG_EPOCH)) + 1;
  if (tid == 0) st_flag(p.ws, flg(FLG_T + par * 32 + h), -1);
  for (int i = 0; i < START_DELAY; i++) __builtin_amdgcn_s_sleep(35);
  u4 Wof[4][8], Wdf[2][8];
  {
    auto issue = [&](int ch) {
      const unsigned dst = (ch & 1) * 65536 + w * 16384;
      static_for<16>([&](auto I) {
        constexpr int i = I.value;
        const int row = w * 16 + i, gran = l ^ ((row & 3) << 2);
        const bf16_t* src = ch < 2 ? p.wo + (size_t)(h * 128 + ch * 64 + row) * 512 + gran * 8 : p.wd + (size_t)((ffn ? h : 0) * 64 + row) * 512 + gran * 8;
        glds16<i * 1024>(src, dst);
      });
    };
    issue(0); issue(1);
    const int q = c >> 2, pp = c & 3;
#pragma unroll
    for (int ch = 0; ch < 3; ch++) {
      if (ch == 0) WAIT_VM(16); else WAIT_VM(0);
      barrier();
      if (ch == 0 && tid == 0) st_tag(p.ws, flg(INIT_T + par * 32 + h), epoch, 1);
#pragma unroll
      for (int ksl = 0; ksl < 2; ksl++)
#pragma unroll
        for (int t = 0; t < 8; t++) {
          const int colb = 256 * w + 64 * (t >> 1) + 16 * pp + 8 * (t & 1);
          u2 hh[2];
#pragma unroll
          for (int n = 0; n < 2; n++) {
            const int row = 32 * ksl + 8 * g + 4 * n + q;
            hh[n] = lds_tr((ch & 1) * 65536 + row * 1024 + (((colb >> 4) ^ (q << 2)) << 4) + (colb & 8));
          }
          const u4 v = {hh[0][0], hh[0][1], hh[1][0], hh[1][1]};
          if (ch < 2) Wof[ch * 2 + ksl][t] = v; else Wdf[ksl][t] = ffn ? v : u4{0, 0, 0, 0};
        }
      barrier();
      if (ch == 0) issue(2);
    }
  }
  constexpr unsigned TB = 28672, oQ = 0, oK = 8192, oV = 16384, oH = 24576, oKM = 2 * TB, oVM = oKM + 2048, oO = oKM + 4096, oRed = oO + 8704;
  auto issueTok = [&](int s) {
    const char* base = p.ws + (size_t)(s * 32 + h) * 8192 + w * 2048 + l * 16;
    const unsigned buf = ((s >> 1) & 1) * TB, dst = buf + w * 2048;
    glds16_sc1<oQ>(base + OFF_Q, dst); glds16_sc1<oQ + 1024>(base + OFF_Q + 1024, dst);
    glds16_sc1<oK>(base + OFF_K, dst); glds16_sc1<oK + 1024>(base + OFF_K + 1024, dst);
    glds16_sc1<oV>(base + OFF_VT, dst); glds16_sc1<oV + 1024>(base + OFF_VT + 1024, dst);
    glds16_sc1<oH>(p.ws + OFF_H + (size_t)(s * 28 + (ffn ? h : 0)) * 4096 + w * 1024 + l * 16, buf + w * 1024);
  };
  const int m = h >> 1;
  const unsigned g1off = (l & 3) == 0 ? flg(FLG_G1 + m) : (l & 3) == 1 ? flg(FLG_G1 + 16 + m) : (l & 3) == 2 ? flg(FLG_G1 + 32 + m) : flg(FLG_G1 + 48 + (ffn ? m : 0));
  const auto rs = __builtin_amdgcn_make_buffer_rsrc(p.ws, 0, WS_USED, 0x00020000);
  const int qt = w & 1, fh = w >> 1, qq = 16 * qt + c;
  float mb[9];
#pragma unroll
  for (int r = 0; r < 8; r++) { mb[r] = 8 * g + r > qq ? -INFINITY : 0.f; asm volatile("" : "+v"(mb[r])); }
  mb[8] = g >= 2 ? -INFINITY : 0.f; asm volatile("" : "+v"(mb[8]));
  unsigned tagv = 0;
  if (!wait_tags(p.ws, l < 4 ? g1off - flg(FLG_G1) + flg(INIT_G1) : l == 4 ? flg(INIT_H + h) : l == 5 ? flg(FLG_SEL) : flg(INIT_T + par * 32 + (l & 31)), l < 6 || l >= 32, epoch, &tagv)) return;
  const unsigned farsel = (unsigned)__builtin_amdgcn_readlane((int)tagv, 5) ^ 1u;
  if (!wait_flags(p.ws, g1off, l < 4, par)) return;
  issueTok(par);
  WAIT_VM(0);
  for (int s = par; s < 64; s += 2) {
    barrier();
    const unsigned tb = ((s >> 1) & 1) * TB;
    f4 st[3] = {f4{0, 0, 0, 0}, f4{0, 0, 0, 0}, f4{0, 0, 0, 0}};
    u4 qf[4], vf[4];
    f4 a[2][8];
    {
      u4 kf[2][4], hf[2][2];
#pragma unroll
      for (int ks = 0; ks < 4; ks++) {
        qf[ks] = lds_rd128(tb + oQ + (qt * 4 + ks) * 1024 + l * 16);
#pragma unroll
        for (int kt = 0; kt < 2; kt++) kf[kt][ks] = lds_rd128(tb + oK + (kt * 4 + ks) * 1024 + l * 16);
      }
#pragma unroll
      for (int tt = 0; tt < 2; tt++)
#pragma unroll
        for (int ks = 0; ks < 2; ks++) hf[tt][ks] = lds_rd128(tb + oH + (tt * 2 + ks) * 1024 + l * 16);
#pragma unroll
      for (int k = 0; k < 4; k++) vf[k] = lds_rd128(tb + oV + (64 * fh + 32 * (k >> 1) + 8 * (c >> 2) + 4 * (k & 1) + (c & 3)) * 64 + g * 16);
      __builtin_amdgcn_sched_barrier(0);
#pragma unroll
      for (int ks = 0; ks < 4; ks++) { st[0] = mfma32(kf[0][ks], qf[ks], st[0]); st[1] = mfma32(kf[1][ks], qf[ks], st[1]); }
#pragma unroll
      for (int tt = 0; tt < 2; tt++)
#pragma unroll
        for (int t = 0; t < 8; t++) {
          a[tt][t] = mfma32(Wdf[0][t], hf[tt][0], f4{0, 0, 0, 0});
          a[tt][t] = mfma32(Wdf[1][t], hf[tt][1], a[tt][t]);
        }
    }
    if (s >= 2) { if (!reduce_tile(p, h, s - 2, oRed, farsel)) return; }
    if (!wait_flags(p.ws, l < 4 ? g1off : flg(FLG_KV + h) + (l & 3) * 4, l < 8, l < 4 ? (s + 2 < 64 ? s + 2 : 63) : s)) return;
    glds16_sc1<oKM>(p.ws + OFF_KVM + (size_t)(s * 32 + h) * 4096 + w * 1024 + l * 16, (unsigned)(w * 1024));
    WAIT_VM(0);
    barrier();
    if (s + 2 < 64) issueTok(s + 2);
    u2 vm[4];
    {
      u4 kmf[4];
#pragma unroll
      for (int ks = 0; ks < 4; ks++) kmf[ks] = lds_rd128(oKM + ks * 512 + g * 128 + (c & 7) * 16);
#pragma unroll
      for (int k = 0; k < 4; k++) vm[k] = lds_rd64(oVM + (64 * fh + 32 * (k >> 1) + 8 * (c >> 2) + 4 * (k & 1) + (c & 3)) * 16 + (g & 1) * 8);
#pragma unroll
      for (int ks = 0; ks < 4; ks++) st[2] = mfma32(kmf[ks], qf[ks], st[2]);
    }
    float mx = -INFINITY;
#pragma unroll
    for (int r = 0; r < 4; r++) {
      st[0][r] += mb[r]; st[1][r] += mb[4 + r]; st[2][r] += mb[8];
      mx = fmaxf(mx, fmaxf(st[0][r], fmaxf(st[1][r], st[2][r])));
    }
    mx = fmaxf(mx, __shfl_xor(mx, 16)); mx = fmaxf(mx, __shfl_xor(mx, 32));
    const float c2 = 0.12751743f;
    float sum = 0;
#pragma unroll
    for (int k = 0; k < 3; k++)
#pragma unroll
      for (int r = 0; r < 4; r++) { st[k][r] = __builtin_amdgcn_exp2f((st[k][r] - mx) * c2); sum += st[k][r]; }
    sum += __shfl_xor(sum, 16); sum += __shfl_xor(sum, 32);
    const float rl = 1.f / sum;
    const u4 pt = {pk2(st[0][0], st[0][1]), pk2(st[0][2], st[0][3]), pk2(st[1][0], st[1][1]), pk2(st[1][2], st[1][3])};
    const u2 pm = {pk2(st[2][0], st[2][1]), pk2(st[2][2], st[2][3])};
#pragma unroll
    for (int P = 0; P < 2; P++) {
      f4 o[2];
#pragma unroll
      for (int e = 0; e < 2; e++) {
        o[e] = mfma32(vf[2 * P + e], pt, f4{0, 0, 0, 0});
        o[e] = mfma16(vm[2 * P + e], pm, o[e]);
      }
      const u4 v = {pk2(o[0][0] * rl, o[0][1] * rl), pk2(o[0][2] * rl, o[0][3] * rl), pk2(o[1][0] * rl, o[1][1] * rl), pk2(o[1][2] * rl, o[1][3] * rl)};
      *(LAS u4*)(uintptr_t)(oO + qq * 272 + (64 * fh + 32 * P + 8 * g) * 2) = v;
    }
    barrier();
    u4 of[2][4];
#pragma unroll
    for (int tt = 0; tt < 2; tt++)
#pragma unroll
      for (int ks = 0; ks < 4; ks++) of[tt][ks] = lds_rd128(oO + (16 * tt + c) * 272 + ks * 64 + g * 16);
    __builtin_amdgcn_sched_barrier(0);
#pragma unroll
    for (int tt = 0; tt < 2; tt++)
#pragma unroll
      for (int P = 0; P < 4; P++) {
#pragma unroll
        for (int e = 0; e < 2; e++)
#pragma unroll
          for (int ks = 0; ks < 4; ks++) a[tt][2 * P + e] = mfma32(Wof[ks][2 * P + e], of[tt][ks], a[tt][2 * P + e]);
        const f4 x0 = a[tt][2 * P], x1 = a[tt][2 * P + 1];
        const u4 v = {pk2(x0[0], x0[1]), pk2(x0[2], x0[3]), pk2(x1[0], x1[1]), pk2(x1[2], x1[3])};
        __builtin_amdgcn_raw_buffer_store_b128(v, rs, part_off((unsigned)(pslot(s) * 32 + h) * 32768u + ((tt * 4 + P) * 4 + w) * 1024, farsel) + l * 16, 0, 16);
      }
    WAIT_VM(0);
    barrier();
    if (tid == 0) st_flag(p.ws, flg(FLG_T + par * 32 + h), s);
    if (s + 2 >= 64) {
      if (!reduce_tile(p, h, s, oRed, farsel)) return;
    }
  }
  if (par == 1 && h == 0) {
    bool ok = wait_tags(p.ws, flg(INIT_G1 + (l < 62 ? l : 0)), true, epoch) && wait_tags(p.ws, flg(INIT_T + l), true, epoch) &&
              wait_tags(p.ws, flg(INIT_H + (l & 31)), true, epoch) && wait_tags(p.ws, flg(INIT_R + l), true, epoch);
    if (ok && tid == 0) st_flag(p.ws, flg(FLG_EPOCH), (int)epoch);
  }
}

__device__ __forceinline__ void head_role(const Params& p, int h) {
  const int w = __builtin_amdgcn_readfirstlane(threadIdx.x >> 6);
  const uint64_t ws = (uint64_t)p.ws, wq = (uint64_t)p.wq, wk = (uint64_t)p.wk, wv = (uint64_t)p.wv, wo = (uint64_t)p.wo, m0 = (uint64_t)p.mem0;
  asm volatile(
    "s_mov_b32 s36, %[ws_lo]\n\t"
    "s_and_b32 s37, %[ws_hi], 0xffff\n\t"
    "s_mov_b32 s38, 214958080\n\t"
    "s_mov_b32 s39, 0x20000\n\t"
    "s_mov_b32 s51, %[w]\n\t"
    "s_mov_b32 s52, %[h]\n\t"
    "s_mov_b32 s42, 8192\n\t"
    "s_mov_b32 s43, 0x20000\n\t"
    "s_mov_b32 s58, 0x00ff00ff\n\t"
    "s_mov_b32 s59, 0x00ff00ff\n\t"
    "s_mov_b32 s60, 0x3e0293ee\n\t"
    "s_mov_b32 s34, 0x3e0293ee\n\t"
    "s_mov_b32 s35, 0x3e0293ee\n\t"
    "s_mov_b32 s29, 12\n\t"
    "s_mov_b32 s61, 0x05040100\n\t"
    "s_mov_b32 s62, 0x07060302\n\t"
    "v_mbcnt_lo_u32_b32 v16, -1, 0\n\t"
    "v_mbcnt_hi_u32_b32 v16, -1, v16\n\t"
    "v_and_b32 v17, 15, v16\n\t"
    "v_lshrrev_b32 v18, 4, v16\n\t"
    "v_and_b32 v19, 1, v18\n\t"
    "v_lshrrev_b32 v20, 5, v16\n\t"
    "v_lshlrev_b32 v0, 4, v16\n\t"
    "s_lshl_b32 s55, s51, 11\n\t"
    "v_add_u32 v1, s55, v0\n\t"
    "v_add_u32 v2, 16384, v1\n\t"
    "v_add_u32 v3, 81920, v1\n\t"
    "s_lshr_b32 s55, s51, 1\n\t"
    "s_mul_i32 s55, s55, 2304\n\t"
    "s_and_b32 s56, s51, 1\n\t"
    "s_lshl_b32 s56, s56, 7\n\t"
    "s_add_u32 s55, s55, s56\n\t"
    "s_add_u32 s55, s55, 8192\n\t"
    "v_mul_u32_u24 v4, 1152, v19\n\t"
    "v_lshl_add_u32 v4, v17, 3, v4\n\t"
    "v_lshl_add_u32 v4, v20, 2, v4\n\t"
    "v_add_u32 v4, s55, v4\n\t"
    "v_and_b32 v21, 7, v17\n\t"
    "v_mul_u32_u24 v22, 288, v21\n\t"
    "v_lshl_add_u32 v5, v18, 4, v22\n\t"
    "v_add_u32 v5, 8192, v5\n\t"
    "s_lshl_b32 s55, s51, 6\n\t"
    "s_add_u32 s56, s55, 12800\n\t"
    "v_lshl_add_u32 v6, v18, 4, v22\n\t"
    "v_add_u32 v6, s56, v6\n\t"
    "v_lshrrev_b32 v23, 3, v17\n\t"
    "v_lshl_add_u32 v7, v18, 5, v22\n\t"
    "v_lshl_add_u32 v7, v23, 4, v7\n\t"
    "v_add_u32 v7, 12800, v7\n\t"
    "s_add_u32 s56, s55, 10496\n\t"
    "v_lshl_add_u32 v13, v18, 4, v22\n\t"
    "v_add_u32 v13, s56, v13\n\t"
    "s_lshl_b32 s56, s51, 9\n\t"
    "v_lshlrev_b32 v12, 4, v21\n\t"
    "v_lshl_add_u32 v12, v18, 7, v12\n\t"
    "v_add_u32 v12, s56, v12\n\t"
    "s_lshl_b32 s56, s51, 9\n\t"
    "s_add_u32 s56, s56, 2048\n\t"
    "v_lshlrev_b32 v14, 5, v17\n\t"
    "v_lshl_add_u32 v14, v20, 4, v14\n\t"
    "v_lshl_add_u32 v14, v19, 3, v14\n\t"
    "v_add_u32 v14, s56, v14\n\t"
    "v_lshl_add_u32 v24, v20, 1, v23\n\t"
    "s_lshl_b32 s56, s51, 3\n\t"
    "v_add_u32 v24, s56, v24\n\t"
    "v_lshlrev_b32 v8, 14, v24\n\t"
    "v_bfe_u32 v24, v17, 2, 1\n\t"
    "v_lshl_add_u32 v8, v24, 12, v8\n\t"
    "v_and_b32 v24, 3, v17\n\t"
    "v_lshl_add_u32 v24, v19, 2, v24\n\t"
    "v_lshl_add_u32 v8, v24, 4, v8\n\t"
    "s_lshl_b32 s56, s52, 7\n\t"
    "v_add_u32 v8, s56, v8\n\t"
    "s_lshl_b32 s56, s51, 11\n\t"
    "s_add_u32 s56, s56, 155648\n\t"
    "v_lshlrev_b32 v10, 7, v17\n\t"
    "v_lshl_add_u32 v10, v18, 4, v10\n\t"
    "v_add_u32 v10, s56, v10\n\t"
    "v_mov_b32 v24, 0x4444\n\t"
    "v_mov_b32 v25, 0xeeee\n\t"
    "v_cmp_lt_u32 vcc, 7, v17\n\t"
    "s_nop 1\n\t"
    "v_cndmask_b32 v11, v24, v25, vcc\n\t"
    "s_lshr_b32 s56, s52, 1\n\t"
    "s_lshl_b32 s56, s56, 7\n\t"
    "s_add_u32 s56, s56, 2097152\n\t"
    "v_mov_b32 v24, s56\n\t"
    "v_add_u32 v25, 2048, v24\n\t"
    "v_cmp_eq_u32 vcc, 1, v16\n\t"
    "s_nop 1\n\t"
    "v_cndmask_b32 v15, v24, v25, vcc\n\t"
    "s_lshl_b32 s56, s52, 7\n\t"
    "s_add_u32 s56, s56, 2113536\n\t"
    "s_lshl_b32 s57, s51, 2\n\t"
    "s_add_u32 s63, s56, s57\n\t"
    "s_mov_b32 s56, 2117888\n\t"
    "buffer_load_dword v30, off, s[36:39], s56 sc1\n\t"
    "v_mov_b32 v31, -1\n\t"
    "buffer_store_dword v31, off, s[36:39], s63 sc1\n\t"
    "s_mov_b32 s40, %[m0_lo]\n\t"
    "s_and_b32 s41, %[m0_hi], 0xffff\n\t"
    "v_lshlrev_b32 v24, 10, v21\n\t"
    "s_lshl_b32 s56, s51, 8\n\t"
    "v_add_u32 v24, s56, v24\n\t"
    "v_lshl_add_u32 v24, v19, 7, v24\n\t"
    "v_lshl_add_u32 v24, v23, 6, v24\n\t"
    "v_lshrrev_b32 v25, 1, v18\n\t"
    "v_lshl_add_u32 v24, v25, 2, v24\n\t"
    "buffer_load_dword v48, v24, s[40:43], 0 offen offset:0\n\t"
    "buffer_load_dword v49, v24, s[40:43], 0 offen offset:16\n\t"
    "buffer_load_dword v50, v24, s[40:43], 0 offen offset:32\n\t"
    "buffer_load_dword v51, v24, s[40:43], 0 offen offset:48\n\t"
    "buffer_load_dword v52, v24, s[40:43], 0 offen offset:8\n\t"
    "buffer_load_dword v53, v24, s[40:43], 0 offen offset:24\n\t"
    "buffer_load_dword v54, v24, s[40:43], 0 offen offset:40\n\t"
    "buffer_load_dword v55, v24, s[40:43], 0 offen offset:56\n\t"
    "v_lshlrev_b32 v26, 13, v18\n\t"
    "v_lshl_add_u32 v26, v17, 4, v26\n\t"
    "v_lshrrev_b32 v25, 1, v18\n\t"
    "v_lshlrev_b32 v27, 6, v25\n\t"
    "v_lshl_add_u32 v27, v19, 4, v27\n\t"
    "s_mov_b32 s64, -1\n\t"
    "s_mov_b32 s71, 0\n\t"
    "s_lshl_b32 s55, s51, 14\n\t"
    "s_add_u32 s55, s55, 16384\n\t"
    "s_lshl_b32 s56, s52, 8\n\t"
    "s_mov_b32 s40, %[wv_lo]\n\t"
    "s_mov_b32 s41, %[wv_hi]\n\t"
    "s_add_u32 s40, s40, s56\n\t"
    "s_addc_u32 s41, s41, 0\n\t"
    "s_sub_u32 s42, 4194304, s56\n\t"
    "s_and_b32 s41, s41, 0xffff\n\t"
    "s_lshl_b32 s56, s51, 19\n\t"
    "s_add_u32 s56, s56, 0\n\t"
    "s_mov_b32 m0, s55\n\t"
    "s_nop 0\n\t"
    "buffer_load_dwordx4 v26, s[40:43], s56 offen lds\n\t"
    "s_add_u32 s55, s55, 1024\n\t"
    "s_add_u32 s56, s56, 32768\n\t"
    "s_mov_b32 m0, s55\n\t"
    "s_nop 0\n\t"
    "buffer_load_dwordx4 v26, s[40:43], s56 offen lds\n\t"
    "s_add_u32 s55, s55, 1024\n\t"
    "s_add_u32 s56, s56, 32768\n\t"
    "s_mov_b32 m0, s55\n\t"
    "s_nop 0\n\t"
    "buffer_load_dwordx4 v26, s[40:43], s56 offen lds\n\t"
    "s_add_u32 s55, s55, 1024\n\t"
    "s_add_u32 s56, s56, 32768\n\t"
    "s_mov_b32 m0, s55\n\t"
    "s_nop 0\n\t"
    "buffer_load_dwordx4 v26, s[40:43], s56 offen lds\n\t"
    "s_add_u32 s55, s55, 1024\n\t"
    "s_add_u32 s56, s56, 32768\n\t"
    "s_mov_b32 m0, s55\n\t"
    "s_nop 0\n\t"
    "buffer_load_dwordx4 v26, s[40:43], s56 offen lds\n\t"
    "s_add_u32 s55, s55, 1024\n\t"
    "s_add_u32 s56, s56, 32768\n\t"
    "s_mov_b32 m0, s55\n\t"
    "s_nop 0\n\t"
    "buffer_load_dwordx4 v26, s[40:43], s56 offen lds\n\t"
    "s_add_u32 s55, s55, 1024\n\t"
    "s_add_u32 s56, s56, 32768\n\t"
    "s_mov_b32 m0, s55\n\t"
    "s_nop 0\n\t"
    "buffer_load_dwordx4 v26, s[40:43], s56 offen lds\n\t"
    "s_add_u32 s55, s55, 1024\n\t"
    "s_add_u32 s56, s56, 32768\n\t"
    "s_mov_b32 m0, s55\n\t"
    "s_nop 0\n\t"
    "buffer_load_dwordx4 v26, s[40:43], s56 offen lds\n\t"
    "s_add_u32 s55, s55, 1024\n\t"
    "s_add_u32 s56, s56, 32768\n\t"
    "s_mov_b32 m0, s55\n\t"
    "s_nop 0\n\t"
    "buffer_load_dwordx4 v26, s[40:43], s56 offen lds\n\t"
    "s_add_u32 s55, s55, 1024\n\t"
    "s_add_u32 s56, s56, 32768\n\t"
    "s_mov_b32 m0, s55\n\t"
    "s_nop 0\n\t"
    "buffer_load_dwordx4 v26, s[40:43], s56 offen lds\n\t"
    "s_add_u32 s55, s55, 1024\n\t"
    "s_add_u32 s56, s56, 32768\n\t"
    "s_mov_b32 m0, s55\n\t"
    "s_nop 0\n\t"
    "buffer_load_dwordx4 v26, s[40:43], s56 offen lds\n\t"
    "s_add_u32 s55, s55, 1024\n\t"
    "s_add_u32 s56, s56, 32768\n\t"
    "s_mov_b32 m0, s55\n\t"
    "s_nop 0\n\t"
    "buffer_load_dwordx4 v26, s[40:43], s56 offen lds\n\t"
    "s_add_u32 s55, s55, 1024\n\t"
    "s_add_u32 s56, s56, 32768\n\t"
    "s_mov_b32 m0, s55\n\t"
    "s_nop 0\n\t"
    "buffer_load_dwordx4 v26, s[40:43], s56 offen lds\n\t"
    "s_add_u32 s55, s55, 1024\n\t"
    "s_add_u32 s56, s56, 32768\n\t"
    "s_mov_b32 m0, s55\n\t"
    "s_nop 0\n\t"
    "buffer_load_dwordx4 v26, s[40:43], s56 offen lds\n\t"
    "s_add_u32 s55, s55, 1024\n\t"
    "s_add_u32 s56, s56, 32768\n\t"
    "s_mov_b32 m0, s55\n\t"
    "s_nop 0\n\t"
    "buffer_load_dwordx4 v26, s[40:43], s56 offen lds\n\t"
    "s_add_u32 s55, s55, 1024\n\t"
    "s_add_u32 s56, s56, 32768\n\t"
    "s_mov_b32 m0, s55\n\t"
    "s_nop 0\n\t"
    "buffer_load_dwordx4 v26, s[40:43], s56 offen lds\n\t"
    "s_lshl_b32 s55, s51, 14\n\t"
    "s_add_u32 s55, s55, 81920\n\t"
    "s_cmp_lt_u32 s51, 2\n\t"
    "s_cselect_b32 s56, %[wq_lo], %[wk_lo]\n\t"
    "s_cselect_b32 s57, %[wq_hi], %[wk_hi]\n\t"
    "s_lshl_b32 s65, s52, 8\n\t"
    "s_mov_b32 s40, s56\n\t"
    "s_mov_b32 s41, s57\n\t"
    "s_add_u32 s40, s40, s65\n\t"
    "s_addc_u32 s41, s41, 0\n\t"
    "s_sub_u32 s42, 4194304, s65\n\t"
    "s_and_b32 s41, s41, 0xffff\n\t"
    "s_and_b32 s56, s51, 1\n\t"
    "s_lshl_b32 s56, s56, 19\n\t"
    "s_add_u32 s56, s56, 0\n\t"
    "s_mov_b32 m0, s55\n\t"
    "s_nop 0\n\t"
    "buffer_load_dwordx4 v26, s[40:43], s56 offen lds\n\t"
    "s_add_u32 s55, s55, 1024\n\t"
    "s_add_u32 s56, s56, 32768\n\t"
    "s_mov_b32 m0, s55\n\t"
    "s_nop 0\n\t"
    "buffer_load_dwordx4 v26, s[40:43], s56 offen lds\n\t"
    "s_add_u32 s55, s55, 1024\n\t"
    "s_add_u32 s56, s56, 32768\n\t"
    "s_mov_b32 m0, s55\n\t"
    "s_nop 0\n\t"
    "buffer_load_dwordx4 v26, s[40:43], s56 offen lds\n\t"
    "s_add_u32 s55, s55, 1024\n\t"
    "s_add_u32 s56, s56, 32768\n\t"
    "s_mov_b32 m0, s55\n\t"
    "s_nop 0\n\t"
    "buffer_load_dwordx4 v26, s[40:43], s56 offen lds\n\t"
    "s_add_u32 s55, s55, 1024\n\t"
    "s_add_u32 s56, s56, 32768\n\t"
    "s_mov_b32 m0, s55\n\t"
    "s_nop 0\n\t"
    "buffer_load_dwordx4 v26, s[40:43], s56 offen lds\n\t"
    "s_add_u32 s55, s55, 1024\n\t"
    "s_add_u32 s56, s56, 32768\n\t"
    "s_mov_b32 m0, s55\n\t"
    "s_nop 0\n\t"
    "buffer_load_dwordx4 v26, s[40:43], s56 offen lds\n\t"
    "s_add_u32 s55, s55, 1024\n\t"
    "s_add_u32 s56, s56, 32768\n\t"
    "s_mov_b32 m0, s55\n\t"
    "s_nop 0\n\t"
    "buffer_load_dwordx4 v26, s[40:43], s56 offen lds\n\t"
    "s_add_u32 s55, s55, 1024\n\t"
    "s_add_u32 s56, s56, 32768\n\t"
    "s_mov_b32 m0, s55\n\t"
    "s_nop 0\n\t"
    "buffer_load_dwordx4 v26, s[40:43], s56 offen lds\n\t"
    "s_add_u32 s55, s55, 1024\n\t"
    "s_add_u32 s56, s56, 32768\n\t"
    "s_mov_b32 m0, s55\n\t"
    "s_nop 0\n\t"
    "buffer_load_dwordx4 v26, s[40:43], s56 offen lds\n\t"
    "s_add_u32 s55, s55, 1024\n\t"
    "s_add_u32 s56, s56, 32768\n\t"
    "s_mov_b32 m0, s55\n\t"
    "s_nop 0\n\t"
    "buffer_load_dwordx4 v26, s[40:43], s56 offen lds\n\t"
    "s_add_u32 s55, s55, 1024\n\t"
    "s_add_u32 s56, s56, 32768\n\t"
    "s_mov_b32 m0, s55\n\t"
    "s_nop 0\n\t"
    "buffer_load_dwordx4 v26, s[40:43], s56 offen lds\n\t"
    "s_add_u32 s55, s55, 1024\n\t"
    "s_add_u32 s56, s56, 32768\n\t"
    "s_mov_b32 m0, s55\n\t"
    "s_nop 0\n\t"
    "buffer_load_dwordx4 v26, s[40:43], s56 offen lds\n\t"
    "s_add_u32 s55, s55, 1024\n\t"
    "s_add_u32 s56, s56, 32768\n\t"
    "s_mov_b32 m0, s55\n\t"
    "s_nop 0\n\t"
    "buffer_load_dwordx4 v26, s[40:43], s56 offen lds\n\t"
    "s_add_u32 s55, s55, 1024\n\t"
    "s_add_u32 s56, s56, 32768\n\t"
    "s_mov_b32 m0, s55\n\t"
    "s_nop 0\n\t"
    "buffer_load_dwordx4 v26, s[40:43], s56 offen lds\n\t"
    "s_add_u32 s55, s55, 1024\n\t"
    "s_add_u32 s56, s56, 32768\n\t"
    "s_mov_b32 m0, s55\n\t"
    "s_nop 0\n\t"
    "buffer_load_dwordx4 v26, s[40:43], s56 offen lds\n\t"
    "s_add_u32 s55, s55, 1024\n\t"
    "s_add_u32 s56, s56, 32768\n\t"
    "s_mov_b32 m0, s55\n\t"
    "s_nop 0\n\t"
    "buffer_load_dwordx4 v26, s[40:43], s56 offen lds\n\t"
    "s_waitcnt vmcnt(16)\n\t"
    "ds_write_b128 v1, v[48:51]\n\t"
    "ds_write_b128 v1, v[52:55] offset:1024\n\t"
    "s_barrier\n\t"
    "v_readfirstlane_b32 s67, v30\n\t"
    "s_add_u32 s67, s67, 1\n\t"
    "s_cmp_eq_u32 s51, 0\n\t"
    "s_cbranch_scc0 HINI_%=\n\t"
    "v_mov_b32 v30, 1\n\t"
    "v_mov_b32 v31, s67\n\t"
    "s_lshl_b32 s56, s52, 7\n\t"
    "s_add_u32 s56, s56, 2138112\n\t"
    "buffer_store_dwordx2 v[30:31], off, s[36:39], s56 sc1\n\t"
    "HINI_%=:\n\t"
    "s_lshl_b32 s55, s51, 6\n\t"
    "s_add_u32 s55, s55, 16384\n\t"
    "v_lshlrev_b32 v28, 8, v27\n\t"
    "v_lshl_add_u32 v28, v17, 2, v28\n\t"
    "v_add_u32 v28, s55, v28\n\t"
    "ds_read_b32 v32, v28 offset:0\n\t"
    "ds_read_b32 v33, v28 offset:256\n\t"
    "ds_read_b32 v34, v28 offset:8192\n\t"
    "ds_read_b32 v35, v28 offset:8448\n\t"
    "ds_read_b32 v36, v28 offset:2048\n\t"
    "ds_read_b32 v37, v28 offset:2304\n\t"
    "ds_read_b32 v38, v28 offset:10240\n\t"
    "ds_read_b32 v39, v28 offset:10496\n\t"
    "ds_read_b32 v40, v28 offset:512\n\t"
    "ds_read_b32 v41, v28 offset:768\n\t"
    "ds_read_b32 v42, v28 offset:8704\n\t"
    "ds_read_b32 v43, v28 offset:8960\n\t"
    "ds_read_b32 v44, v28 offset:2560\n\t"
    "ds_read_b32 v45, v28 offset:2816\n\t"
    "ds_read_b32 v46, v28 offset:10752\n\t"
    "ds_read_b32 v47, v28 offset:11008\n\t"
    "s_waitcnt lgkmcnt(14)\n\t"
    "v_perm_b32 v29, v33, v32, s61\n\t"
    "v_accvgpr_write_b32 a192, v29\n\t"
    "s_waitcnt lgkmcnt(12)\n\t"
    "v_perm_b32 v29, v35, v34, s61\n\t"
    "v_accvgpr_write_b32 a193, v29\n\t"
    "s_waitcnt lgkmcnt(10)\n\t"
    "v_perm_b32 v29, v37, v36, s61\n\t"
    "v_accvgpr_write_b32 a194, v29\n\t"
    "s_waitcnt lgkmcnt(8)\n\t"
    "v_perm_b32 v29, v39, v38, s61\n\t"
    "v_accvgpr_write_b32 a195, v29\n\t"
    "s_waitcnt lgkmcnt(6)\n\t"
    "v_perm_b32 v29, v41, v40, s61\n\t"
    "v_accvgpr_write_b32 a196, v29\n\t"
    "s_waitcnt lgkmcnt(4)\n\t"
    "v_perm_b32 v29, v43, v42, s61\n\t"
    "v_accvgpr_write_b32 a197, v29\n\t"
    "s_waitcnt lgkmcnt(2)\n\t"
    "v_perm_b32 v29, v45, v44, s61\n\t"
    "v_accvgpr_write_b32 a198, v29\n\t"
    "s_waitcnt lgkmcnt(0)\n\t"
    "v_perm_b32 v29, v47, v46, s61\n\t"
    "v_accvgpr_write_b32 a199, v29\n\t"
    "v_perm_b32 v29, v33, v32, s62\n\t"
    "v_accvgpr_write_b32 a200, v29\n\t"
    "v_perm_b32 v29, v35, v34, s62\n\t"
    "v_accvgpr_write_b32 a201, v29\n\t"
    "v_perm_b32 v29, v37, v36, s62\n\t"
    "v_accvgpr_write_b32 a202, v29\n\t"
    "v_perm_b32 v29, v39, v38, s62\n\t"
    "v_accvgpr_write_b32 a203, v29\n\t"
    "v_perm_b32 v29, v41, v40, s62\n\t"
    "v_accvgpr_write_b32 a204, v29\n\t"
    "v_perm_b32 v29, v43, v42, s62\n\t"
    "v_accvgpr_write_b32 a205, v29\n\t"
    "v_perm_b32 v29, v45, v44, s62\n\t"
    "v_accvgpr_write_b32 a206, v29\n\t"
    "v_perm_b32 v29, v47, v46, s62\n\t"
    "v_accvgpr_write_b32 a207, v29\n\t"
    "ds_read_b32 v32, v28 offset:1024\n\t"
    "ds_read_b32 v33, v28 offset:1280\n\t"
    "ds_read_b32 v34, v28 offset:9216\n\t"
    "ds_read_b32 v35, v28 offset:9472\n\t"
    "ds_read_b32 v36, v28 offset:3072\n\t"
    "ds_read_b32 v37, v28 offset:3328\n\t"
    "ds_read_b32 v38, v28 offset:11264\n\t"
    "ds_read_b32 v39, v28 offset:11520\n\t"
    "ds_read_b32 v40, v28 offset:1536\n\t"
    "ds_read_b32 v41, v28 offset:1792\n\t"
    "ds_read_b32 v42, v28 offset:9728\n\t"
    "ds_read_b32 v43, v28 offset:9984\n\t"
    "ds_read_b32 v44, v28 offset:3584\n\t"
    "ds_read_b32 v45, v28 offset:3840\n\t"
    "ds_read_b32 v46, v28 offset:11776\n\t"
    "ds_read_b32 v47, v28 offset:12032\n\t"
    "s_waitcnt lgkmcnt(14)\n\t"
    "v_perm_b32 v29, v33, v32, s61\n\t"
    "v_accvgpr_write_b32 a208, v29\n\t"
    "s_waitcnt lgkmcnt(12)\n\t"
    "v_perm_b32 v29, v35, v34, s61\n\t"
    "v_accvgpr_write_b32 a209, v29\n\t"
    "s_waitcnt lgkmcnt(10)\n\t"
    "v_perm_b32 v29, v37, v36, s61\n\t"
    "v_accvgpr_write_b32 a210, v29\n\t"
    "s_waitcnt lgkmcnt(8)\n\t"
    "v_perm_b32 v29, v39, v38, s61\n\t"
    "v_accvgpr_write_b32 a211, v29\n\t"
    "s_waitcnt lgkmcnt(6)\n\t"
    "v_perm_b32 v29, v41, v40, s61\n\t"
    "v_accvgpr_write_b32 a212, v29\n\t"
    "s_waitcnt lgkmcnt(4)\n\t"
    "v_perm_b32 v29, v43, v42, s61\n\t"
    "v_accvgpr_write_b32 a213, v29\n\t"
    "s_waitcnt lgkmcnt(2)\n\t"
    "v_perm_b32 v29, v45, v44, s61\n\t"
    "v_accvgpr_write_b32 a214, v29\n\t"
    "s_waitcnt lgkmcnt(0)\n\t"
    "v_perm_b32 v29, v47, v46, s61\n\t"
    "v_accvgpr_write_b32 a215, v29\n\t"
    "v_perm_b32 v29, v33, v32, s62\n\t"
    "v_accvgpr_write_b32 a216, v29\n\t"
    "v_perm_b32 v29, v35, v34, s62\n\t"
    "v_accvgpr_write_b32 a217, v29\n\t"
    "v_perm_b32 v29, v37, v36, s62\n\t"
    "v_accvgpr_write_b32 a218, v29\n\t"
    "v_perm_b32 v29, v39, v38, s62\n\t"
    "v_accvgpr_write_b32 a219, v29\n\t"
    "v_perm_b32 v29, v41, v40, s62\n\t"
    "v_accvgpr_write_b32 a220, v29\n\t"
    "v_perm_b32 v29, v43, v42, s62\n\t"
    "v_accvgpr_write_b32 a221, v29\n\t"
    "v_perm_b32 v29, v45, v44, s62\n\t"
    "v_accvgpr_write_b32 a222, v29\n\t"
    "v_perm_b32 v29, v47, v46, s62\n\t"
    "v_accvgpr_write_b32 a223, v29\n\t"
    "ds_read_b32 v32, v28 offset:32768\n\t"
    "ds_read_b32 v33, v28 offset:33024\n\t"
    "ds_read_b32 v34, v28 offset:40960\n\t"
    "ds_read_b32 v35, v28 offset:41216\n\t"
    "ds_read_b32 v36, v28 offset:34816\n\t"
    "ds_read_b32 v37, v28 offset:35072\n\t"
    "ds_read_b32 v38, v28 offset:43008\n\t"
    "ds_read_b32 v39, v28 offset:43264\n\t"
    "ds_read_b32 v40, v28 offset:33280\n\t"
    "ds_read_b32 v41, v28 offset:33536\n\t"
    "ds_read_b32 v42, v28 offset:41472\n\t"
    "ds_read_b32 v43, v28 offset:41728\n\t"
    "ds_read_b32 v44, v28 offset:35328\n\t"
    "ds_read_b32 v45, v28 offset:35584\n\t"
    "ds_read_b32 v46, v28 offset:43520\n\t"
    "ds_read_b32 v47, v28 offset:43776\n\t"
    "s_waitcnt lgkmcnt(14)\n\t"
    "v_perm_b32 v29, v33, v32, s61\n\t"
    "v_accvgpr_write_b32 a224, v29\n\t"
    "s_waitcnt lgkmcnt(12)\n\t"
    "v_perm_b32 v29, v35, v34, s61\n\t"
    "v_accvgpr_write_b32 a225, v29\n\t"
    "s_waitcnt lgkmcnt(10)\n\t"
    "v_perm_b32 v29, v37, v36, s61\n\t"
    "v_accvgpr_write_b32 a226, v29\n\t"
    "s_waitcnt lgkmcnt(8)\n\t"
    "v_perm_b32 v29, v39, v38, s61\n\t"
    "v_accvgpr_write_b32 a227, v29\n\t"
    "s_waitcnt lgkmcnt(6)\n\t"
    "v_perm_b32 v29, v41, v40, s61\n\t"
    "v_accvgpr_write_b32 a228, v29\n\t"
    "s_waitcnt lgkmcnt(4)\n\t"
    "v_perm_b32 v29, v43, v42, s61\n\t"
    "v_accvgpr_write_b32 a229, v29\n\t"
    "s_waitcnt lgkmcnt(2)\n\t"
    "v_perm_b32 v29, v45, v44, s61\n\t"
    "v_accvgpr_write_b32 a230, v29\n\t"
    "s_waitcnt lgkmcnt(0)\n\t"
    "v_perm_b32 v29, v47, v46, s61\n\t"
    "v_accvgpr_write_b32 a231, v29\n\t"
    "v_perm_b32 v29, v33, v32, s62\n\t"
    "v_accvgpr_write_b32 a232, v29\n\t"
    "v_perm_b32 v29, v35, v34, s62\n\t"
    "v_accvgpr_write_b32 a233, v29\n\t"
    "v_perm_b32 v29, v37, v36, s62\n\t"
    "v_accvgpr_write_b32 a234, v29\n\t"
    "v_perm_b32 v29, v39, v38, s62\n\t"
    "v_accvgpr_write_b32 a235, v29\n\t"
    "v_perm_b32 v29, v41, v40, s62\n\t"
    "v_accvgpr_write_b32 a236, v29\n\t"
    "v_perm_b32 v29, v43, v42, s62\n\t"
    "v_accvgpr_write_b32 a237, v29\n\t"
    "v_perm_b32 v29, v45, v44, s62\n\t"
    "v_accvgpr_write_b32 a238, v29\n\t"
    "v_perm_b32 v29, v47, v46, s62\n\t"
    "v_accvgpr_write_b32 a239, v29\n\t"
    "ds_read_b32 v32, v28 offset:33792\n\t"
    "ds_read_b32 v33, v28 offset:34048\n\t"
    "ds_read_b32 v34, v28 offset:41984\n\t"
    "ds_read_b32 v35, v28 offset:42240\n\t"
    "ds_read_b32 v36, v28 offset:35840\n\t"
    "ds_read_b32 v37, v28 offset:36096\n\t"
    "ds_read_b32 v38, v28 offset:44032\n\t"
    "ds_read_b32 v39, v28 offset:44288\n\t"
    "ds_read_b32 v40, v28 offset:34304\n\t"
    "ds_read_b32 v41, v28 offset:34560\n\t"
    "ds_read_b32 v42, v28 offset:42496\n\t"
    "ds_read_b32 v43, v28 offset:42752\n\t"
    "ds_read_b32 v44, v28 offset:36352\n\t"
    "ds_read_b32 v45, v28 offset:36608\n\t"
    "ds_read_b32 v46, v28 offset:44544\n\t"
    "ds_read_b32 v47, v28 offset:44800\n\t"
    "s_waitcnt lgkmcnt(14)\n\t"
    "v_perm_b32 v29, v33, v32, s61\n\t"
    "v_accvgpr_write_b32 a240, v29\n\t"
    "s_waitcnt lgkmcnt(12)\n\t"
    "v_perm_b32 v29, v35, v34, s61\n\t"
    "v_accvgpr_write_b32 a241, v29\n\t"
    "s_waitcnt lgkmcnt(10)\n\t"
    "v_perm_b32 v29, v37, v36, s61\n\t"
    "v_accvgpr_write_b32 a242, v29\n\t"
    "s_waitcnt lgkmcnt(8)\n\t"
    "v_perm_b32 v29, v39, v38, s61\n\t"
    "v_accvgpr_write_b32 a243, v29\n\t"
    "s_waitcnt lgkmcnt(6)\n\t"
    "v_perm_b32 v29, v41, v40, s61\n\t"
    "v_accvgpr_write_b32 a244, v29\n\t"
    "s_waitcnt lgkmcnt(4)\n\t"
    "v_perm_b32 v29, v43, v42, s61\n\t"
    "v_accvgpr_write_b32 a245, v29\n\t"
    "s_waitcnt lgkmcnt(2)\n\t"
    "v_perm_b32 v29, v45, v44, s61\n\t"
    "v_accvgpr_write_b32 a246, v29\n\t"
    "s_waitcnt lgkmcnt(0)\n\t"
    "v_perm_b32 v29, v47, v46, s61\n\t"
    "v_accvgpr_write_b32 a247, v29\n\t"
    "v_perm_b32 v29, v33, v32, s62\n\t"
    "v_accvgpr_write_b32 a248, v29\n\t"
    "v_perm_b32 v29, v35, v34, s62\n\t"
    "v_accvgpr_write_b32 a249, v29\n\t"
    "v_perm_b32 v29, v37, v36, s62\n\t"
    "v_accvgpr_write_b32 a250, v29\n\t"
    "v_perm_b32 v29, v39, v38, s62\n\t"
    "v_accvgpr_write_b32 a251, v29\n\t"
    "v_perm_b32 v29, v41, v40, s62\n\t"
    "v_accvgpr_write_b32 a252, v29\n\t"
    "v_perm_b32 v29, v43, v42, s62\n\t"
    "v_accvgpr_write_b32 a253, v29\n\t"
    "v_perm_b32 v29, v45, v44, s62\n\t"
    "v_accvgpr_write_b32 a254, v29\n\t"
    "v_perm_b32 v29, v47, v46, s62\n\t"
    "v_accvgpr_write_b32 a255, v29\n\t"
    "s_waitcnt lgkmcnt(0)\n\t"
    "s_barrier\n\t"
    "s_lshl_b32 s55, s51, 14\n\t"
    "s_add_u32 s55, s55, 16384\n\t"
    "s_cmp_lt_u32 s51, 2\n\t"
    "s_cselect_b32 s56, %[wq_lo], %[wk_lo]\n\t"
    "s_cselect_b32 s57, %[wq_hi], %[wk_hi]\n\t"
    "s_lshl_b32 s65, s52, 8\n\t"
    "s_mov_b32 s40, s56\n\t"
    "s_mov_b32 s41, s57\n\t"
    "s_add_u32 s40, s40, s65\n\t"
    "s_addc_u32 s41, s41, 0\n\t"
    "s_sub_u32 s42, 4194304, s65\n\t"
    "s_and_b32 s41, s41, 0xffff\n\t"
    "s_and_b32 s56, s51, 1\n\t"
    "s_lshl_b32 s56, s56, 19\n\t"
    "s_add_u32 s56, s56, 1048576\n\t"
    "s_mov_b32 m0, s55\n\t"
    "s_nop 0\n\t"
    "buffer_load_dwordx4 v26, s[40:43], s56 offen lds\n\t"
    "s_add_u32 s55, s55, 1024\n\t"
    "s_add_u32 s56, s56, 32768\n\t"
    "s_mov_b32 m0, s55\n\t"
    "s_nop 0\n\t"
    "buffer_load_dwordx4 v26, s[40:43], s56 offen lds\n\t"
    "s_add_u32 s55, s55, 1024\n\t"
    "s_add_u32 s56, s56, 32768\n\t"
    "s_mov_b32 m0, s55\n\t"
    "s_nop 0\n\t"
    "buffer_load_dwordx4 v26, s[40:43], s56 offen lds\n\t"
    "s_add_u32 s55, s55, 1024\n\t"
    "s_add_u32 s56, s56, 32768\n\t"
    "s_mov_b32 m0, s55\n\t"
    "s_nop 0\n\t"
    "buffer_load_dwordx4 v26, s[40:43], s56 offen lds\n\t"
    "s_add_u32 s55, s55, 1024\n\t"
    "s_add_u32 s56, s56, 32768\n\t"
    "s_mov_b32 m0, s55\n\t"
    "s_nop 0\n\t"
    "buffer_load_dwordx4 v26, s[40:43], s56 offen lds\n\t"
    "s_add_u32 s55, s55, 1024\n\t"
    "s_add_u32 s56, s56, 32768\n\t"
    "s_mov_b32 m0, s55\n\t"
    "s_nop 0\n\t"
    "buffer_load_dwordx4 v26, s[40:43], s56 offen lds\n\t"
    "s_add_u32 s55, s55, 1024\n\t"
    "s_add_u32 s56, s56, 32768\n\t"
    "s_mov_b32 m0, s55\n\t"
    "s_nop 0\n\t"
    "buffer_load_dwordx4 v26, s[40:43], s56 offen lds\n\t"
    "s_add_u32 s55, s55, 1024\n\t"
    "s_add_u32 s56, s56, 32768\n\t"
    "s_mov_b32 m0, s55\n\t"
    "s_nop 0\n\t"
    "buffer_load_dwordx4 v26, s[40:43], s56 offen lds\n\t"
    "s_add_u32 s55, s55, 1024\n\t"
    "s_add_u32 s56, s56, 32768\n\t"
    "s_mov_b32 m0, s55\n\t"
    "s_nop 0\n\t"
    "buffer_load_dwordx4 v26, s[40:43], s56 offen lds\n\t"
    "s_add_u32 s55, s55, 1024\n\t"
    "s_add_u32 s56, s56, 32768\n\t"
    "s_mov_b32 m0, s55\n\t"
    "s_nop 0\n\t"
    "buffer_load_dwordx4 v26, s[40:43], s56 offen lds\n\t"
    "s_add_u32 s55, s55, 1024\n\t"
    "s_add_u32 s56, s56, 32768\n\t"
    "s_mov_b32 m0, s55\n\t"
    "s_nop 0\n\t"
    "buffer_load_dwordx4 v26, s[40:43], s56 offen lds\n\t"
    "s_add_u32 s55, s55, 1024\n\t"
    "s_add_u32 s56, s56, 32768\n\t"
    "s_mov_b32 m0, s55\n\t"
    "s_nop 0\n\t"
    "buffer_load_dwordx4 v26, s[40:43], s56 offen lds\n\t"
    "s_add_u32 s55, s55, 1024\n\t"
    "s_add_u32 s56, s56, 32768\n\t"
    "s_mov_b32 m0, s55\n\t"
    "s_nop 0\n\t"
    "buffer_load_dwordx4 v26, s[40:43], s56 offen lds\n\t"
    "s_add_u32 s55, s55, 1024\n\t"
    "s_add_u32 s56, s56, 32768\n\t"
    "s_mov_b32 m0, s55\n\t"
    "s_nop 0\n\t"
    "buffer_load_dwordx4 v26, s[40:43], s56 offen lds\n\t"
    "s_add_u32 s55, s55, 1024\n\t"
    "s_add_u32 s56, s56, 32768\n\t"
    "s_mov_b32 m0, s55\n\t"
    "s_nop 0\n\t"
    "buffer_load_dwordx4 v26, s[40:43], s56 offen lds\n\t"
    "s_add_u32 s55, s55, 1024\n\t"
    "s_add_u32 s56, s56, 32768\n\t"
    "s_mov_b32 m0, s55\n\t"
    "s_nop 0\n\t"
    "buffer_load_dwordx4 v26, s[40:43], s56 offen lds\n\t"
    "s_waitcnt vmcnt(16)\n\t"
    "s_barrier\n\t"
    "s_lshr_b32 s55, s51, 1\n\t"
    "s_lshl_b32 s55, s55, 15\n\t"
    "s_and_b32 s56, s51, 1\n\t"
    "s_lshl_b32 s56, s56, 7\n\t"
    "s_add_u32 s55, s55, s56\n\t"
    "s_add_u32 s55, s55, 81920\n\t"
    "v_lshlrev_b32 v28, 8, v27\n\t"
    "v_lshl_add_u32 v28, v17, 3, v28\n\t"
    "v_add_u32 v28, s55, v28\n\t"
    "ds_read_b64 v[32:33], v28 offset:0\n\t"
    "ds_read_b64 v[34:35], v28 offset:256\n\t"
    "ds_read_b64 v[36:37], v28 offset:8192\n\t"
    "ds_read_b64 v[38:39], v28 offset:8448\n\t"
    "ds_read_b64 v[40:41], v28 offset:2048\n\t"
    "ds_read_b64 v[42:43], v28 offset:2304\n\t"
    "ds_read_b64 v[44:45], v28 offset:10240\n\t"
    "ds_read_b64 v[46:47], v28 offset:10496\n\t"
    "ds_read_b64 v[48:49], v28 offset:512\n\t"
    "ds_read_b64 v[50:51], v28 offset:768\n\t"
    "ds_read_b64 v[52:53], v28 offset:8704\n\t"
    "ds_read_b64 v[54:55], v28 offset:8960\n\t"
    "ds_read_b64 v[56:57], v28 offset:2560\n\t"
    "ds_read_b64 v[58:59], v28 offset:2816\n\t"
    "ds_read_b64 v[60:61], v28 offset:10752\n\t"
    "ds_read_b64 v[62:63], v28 offset:11008\n\t"
    "s_waitcnt lgkmcnt(14)\n\t"
    "v_perm_b32 v29, v34, v32, s61\n\t"
    "v_accvgpr_write_b32 a0, v29\n\t"
    "s_waitcnt lgkmcnt(12)\n\t"
    "v_perm_b32 v29, v38, v36, s61\n\t"
    "v_accvgpr_write_b32 a1, v29\n\t"
    "s_waitcnt lgkmcnt(10)\n\t"
    "v_perm_b32 v29, v42, v40, s61\n\t"
    "v_accvgpr_write_b32 a2, v29\n\t"
    "s_waitcnt lgkmcnt(8)\n\t"
    "v_perm_b32 v29, v46, v44, s61\n\t"
    "v_accvgpr_write_b32 a3, v29\n\t"
    "s_waitcnt lgkmcnt(6)\n\t"
    "v_perm_b32 v29, v50, v48, s61\n\t"
    "v_accvgpr_write_b32 a4, v29\n\t"
    "s_waitcnt lgkmcnt(4)\n\t"
    "v_perm_b32 v29, v54, v52, s61\n\t"
    "v_accvgpr_write_b32 a5, v29\n\t"
    "s_waitcnt lgkmcnt(2)\n\t"
    "v_perm_b32 v29, v58, v56, s61\n\t"
    "v_accvgpr_write_b32 a6, v29\n\t"
    "s_waitcnt lgkmcnt(0)\n\t"
    "v_perm_b32 v29, v62, v60, s61\n\t"
    "v_accvgpr_write_b32 a7, v29\n\t"
    "v_perm_b32 v29, v34, v32, s62\n\t"
    "v_accvgpr_write_b32 a64, v29\n\t"
    "v_perm_b32 v29, v38, v36, s62\n\t"
    "v_accvgpr_write_b32 a65, v29\n\t"
    "v_perm_b32 v29, v42, v40, s62\n\t"
    "v_accvgpr_write_b32 a66, v29\n\t"
    "v_perm_b32 v29, v46, v44, s62\n\t"
    "v_accvgpr_write_b32 a67, v29\n\t"
    "v_perm_b32 v29, v50, v48, s62\n\t"
    "v_accvgpr_write_b32 a68, v29\n\t"
    "v_perm_b32 v29, v54, v52, s62\n\t"
    "v_accvgpr_write_b32 a69, v29\n\t"
    "v_perm_b32 v29, v58, v56, s62\n\t"
    "v_accvgpr_write_b32 a70, v29\n\t"
    "v_perm_b32 v29, v62, v60, s62\n\t"
    "v_accvgpr_write_b32 a71, v29\n\t"
    "v_perm_b32 v29, v35, v33, s61\n\t"
    "v_accvgpr_write_b32 a128, v29\n\t"
    "v_perm_b32 v29, v39, v37, s61\n\t"
    "v_accvgpr_write_b32 a129, v29\n\t"
    "v_perm_b32 v29, v43, v41, s61\n\t"
    "v_accvgpr_write_b32 a130, v29\n\t"
    "v_perm_b32 v29, v47, v45, s61\n\t"
    "v_accvgpr_write_b32 a131, v29\n\t"
    "v_perm_b32 v29, v51, v49, s61\n\t"
    "v_accvgpr_write_b32 a132, v29\n\t"
    "v_perm_b32 v29, v55, v53, s61\n\t"
    "v_accvgpr_write_b32 a133, v29\n\t"
    "v_perm_b32 v29, v59, v57, s61\n\t"
    "v_accvgpr_write_b32 a134, v29\n\t"
    "v_perm_b32 v29, v63, v61, s61\n\t"
    "v_accvgpr_write_b32 a135, v29\n\t"
    "v_perm_b32 v64, v35, v33, s62\n\t"
    "v_perm_b32 v65, v39, v37, s62\n\t"
    "v_perm_b32 v66, v43, v41, s62\n\t"
    "v_perm_b32 v67, v47, v45, s62\n\t"
    "v_perm_b32 v68, v51, v49, s62\n\t"
    "v_perm_b32 v69, v55, v53, s62\n\t"
    "v_perm_b32 v70, v59, v57, s62\n\t"
    "v_perm_b32 v71, v63, v61, s62\n\t"
    "ds_read_b64 v[32:33], v28 offset:1024\n\t"
    "ds_read_b64 v[34:35], v28 offset:1280\n\t"
    "ds_read_b64 v[36:37], v28 offset:9216\n\t"
    "ds_read_b64 v[38:39], v28 offset:9472\n\t"
    "ds_read_b64 v[40:41], v28 offset:3072\n\t"
    "ds_read_b64 v[42:43], v28 offset:3328\n\t"
    "ds_read_b64 v[44:45], v28 offset:11264\n\t"
    "ds_read_b64 v[46:47], v28 offset:11520\n\t"
    "ds_read_b64 v[48:49], v28 offset:1536\n\t"
    "ds_read_b64 v[50:51], v28 offset:1792\n\t"
    "ds_read_b64 v[52:53], v28 offset:9728\n\t"
    "ds_read_b64 v[54:55], v28 offset:9984\n\t"
    "ds_read_b64 v[56:57], v28 offset:3584\n\t"
    "ds_read_b64 v[58:59], v28 offset:3840\n\t"
    "ds_read_b64 v[60:61], v28 offset:11776\n\t"
    "ds_read_b64 v[62:63], v28 offset:12032\n\t"
    "s_waitcnt lgkmcnt(14)\n\t"
    "v_perm_b32 v29, v34, v32, s61\n\t"
    "v_accvgpr_write_b32 a8, v29\n\t"
    "s_waitcnt lgkmcnt(12)\n\t"
    "v_perm_b32 v29, v38, v36, s61\n\t"
    "v_accvgpr_write_b32 a9, v29\n\t"
    "s_waitcnt lgkmcnt(10)\n\t"
    "v_perm_b32 v29, v42, v40, s61\n\t"
    "v_accvgpr_write_b32 a10, v29\n\t"
    "s_waitcnt lgkmcnt(8)\n\t"
    "v_perm_b32 v29, v46, v44, s61\n\t"
    "v_accvgpr_write_b32 a11, v29\n\t"
    "s_waitcnt lgkmcnt(6)\n\t"
    "v_perm_b32 v29, v50, v48, s61\n\t"
    "v_accvgpr_write_b32 a12, v29\n\t"
    "s_waitcnt lgkmcnt(4)\n\t"
    "v_perm_b32 v29, v54, v52, s61\n\t"
    "v_accvgpr_write_b32 a13, v29\n\t"
    "s_waitcnt lgkmcnt(2)\n\t"
    "v_perm_b32 v29, v58, v56, s61\n\t"
    "v_accvgpr_write_b32 a14, v29\n\t"
    "s_waitcnt lgkmcnt(0)\n\t"
    "v_perm_b32 v29, v62, v60, s61\n\t"
    "v_accvgpr_write_b32 a15, v29\n\t"
    "v_perm_b32 v29, v34, v32, s62\n\t"
    "v_accvgpr_write_b32 a72, v29\n\t"
    "v_perm_b32 v29, v38, v36, s62\n\t"
    "v_accvgpr_write_b32 a73, v29\n\t"
    "v_perm_b32 v29, v42, v40, s62\n\t"
    "v_accvgpr_write_b32 a74, v29\n\t"
    "v_perm_b32 v29, v46, v44, s62\n\t"
    "v_accvgpr_write_b32 a75, v29\n\t"
    "v_perm_b32 v29, v50, v48, s62\n\t"
    "v_accvgpr_write_b32 a76, v29\n\t"
    "v_perm_b32 v29, v54, v52, s62\n\t"
    "v_accvgpr_write_b32 a77, v29\n\t"
    "v_perm_b32 v29, v58, v56, s62\n\t"
    "v_accvgpr_write_b32 a78, v29\n\t"
    "v_perm_b32 v29, v62, v60, s62\n\t"
    "v_accvgpr_write_b32 a79, v29\n\t"
    "v_perm_b32 v29, v35, v33, s61\n\t"
    "v_accvgpr_write_b32 a136, v29\n\t"
    "v_perm_b32 v29, v39, v37, s61\n\t"
    "v_accvgpr_write_b32 a137, v29\n\t"
    "v_perm_b32 v29, v43, v41, s61\n\t"
    "v_accvgpr_write_b32 a138, v29\n\t"
    "v_perm_b32 v29, v47, v45, s61\n\t"
    "v_accvgpr_write_b32 a139, v29\n\t"
    "v_perm_b32 v29, v51, v49, s61\n\t"
    "v_accvgpr_write_b32 a140, v29\n\t"
    "v_perm_b32 v29, v55, v53, s61\n\t"
    "v_accvgpr_write_b32 a141, v29\n\t"
    "v_perm_b32 v29, v59, v57, s61\n\t"
    "v_accvgpr_write_b32 a142, v29\n\t"
    "v_perm_b32 v29, v63, v61, s61\n\t"
    "v_accvgpr_write_b32 a143, v29\n\t"
    "v_perm_b32 v72, v35, v33, s62\n\t"
    "v_perm_b32 v73, v39, v37, s62\n\t"
    "v_perm_b32 v74, v43, v41, s62\n\t"
    "v_perm_b32 v75, v47, v45, s62\n\t"
    "v_perm_b32 v76, v51, v49, s62\n\t"
    "v_perm_b32 v77, v55, v53, s62\n\t"
    "v_perm_b32 v78, v59, v57, s62\n\t"
    "v_perm_b32 v79, v63, v61, s62\n\t"
    "s_waitcnt lgkmcnt(0)\n\t"
    "s_barrier\n\t"
    "s_lshl_b32 s55, s51, 14\n\t"
    "s_add_u32 s55, s55, 81920\n\t"
    "s_cmp_lt_u32 s51, 2\n\t"
    "s_cselect_b32 s56, %[wq_lo], %[wk_lo]\n\t"
    "s_cselect_b32 s57, %[wq_hi], %[wk_hi]\n\t"
    "s_lshl_b32 s65, s52, 8\n\t"
    "s_mov_b32 s40, s56\n\t"
    "s_mov_b32 s41, s57\n\t"
    "s_add_u32 s40, s40, s65\n\t"
    "s_addc_u32 s41, s41, 0\n\t"
    "s_sub_u32 s42, 4194304, s65\n\t"
    "s_and_b32 s41, s41, 0xffff\n\t"
    "s_and_b32 s56, s51, 1\n\t"
    "s_lshl_b32 s56, s56, 19\n\t"
    "s_add_u32 s56, s56, 2097152\n\t"
    "s_mov_b32 m0, s55\n\t"
    "s_nop 0\n\t"
    "buffer_load_dwordx4 v26, s[40:43], s56 offen lds\n\t"
    "s_add_u32 s55, s55, 1024\n\t"
    "s_add_u32 s56, s56, 32768\n\t"
    "s_mov_b32 m0, s55\n\t"
    "s_nop 0\n\t"
    "buffer_load_dwordx4 v26, s[40:43], s56 offen lds\n\t"
    "s_add_u32 s55, s55, 1024\n\t"
    "s_add_u32 s56, s56, 32768\n\t"
    "s_mov_b32 m0, s55\n\t"
    "s_nop 0\n\t"
    "buffer_load_dwordx4 v26, s[40:43], s56 offen lds\n\t"
    "s_add_u32 s55, s55, 1024\n\t"
    "s_add_u32 s56, s56, 32768\n\t"
    "s_mov_b32 m0, s55\n\t"
    "s_nop 0\n\t"
    "buffer_load_dwordx4 v26, s[40:43], s56 offen lds\n\t"
    "s_add_u32 s55, s55, 1024\n\t"
    "s_add_u32 s56, s56, 32768\n\t"
    "s_mov_b32 m0, s55\n\t"
    "s_nop 0\n\t"
    "buffer_load_dwordx4 v26, s[40:43], s56 offen lds\n\t"
    "s_add_u32 s55, s55, 1024\n\t"
    "s_add_u32 s56, s56, 32768\n\t"
    "s_mov_b32 m0, s55\n\t"
    "s_nop 0\n\t"
    "buffer_load_dwordx4 v26, s[40:43], s56 offen lds\n\t"
    "s_add_u32 s55, s55, 1024\n\t"
    "s_add_u32 s56, s56, 32768\n\t"
    "s_mov_b32 m0, s55\n\t"
    "s_nop 0\n\t"
    "buffer_load_dwordx4 v26, s[40:43], s56 offen lds\n\t"
    "s_add_u32 s55, s55, 1024\n\t"
    "s_add_u32 s56, s56, 32768\n\t"
    "s_mov_b32 m0, s55\n\t"
    "s_nop 0\n\t"
    "buffer_load_dwordx4 v26, s[40:43], s56 offen lds\n\t"
    "s_add_u32 s55, s55, 1024\n\t"
    "s_add_u32 s56, s56, 32768\n\t"
    "s_mov_b32 m0, s55\n\t"
    "s_nop 0\n\t"
    "buffer_load_dwordx4 v26, s[40:43], s56 offen lds\n\t"
    "s_add_u32 s55, s55, 1024\n\t"
    "s_add_u32 s56, s56, 32768\n\t"
    "s_mov_b32 m0, s55\n\t"
    "s_nop 0\n\t"
    "buffer_load_dwordx4 v26, s[40:43], s56 offen lds\n\t"
    "s_add_u32 s55, s55, 1024\n\t"
    "s_add_u32 s56, s56, 32768\n\t"
    "s_mov_b32 m0, s55\n\t"
    "s_nop 0\n\t"
    "buffer_load_dwordx4 v26, s[40:43], s56 offen lds\n\t"
    "s_add_u32 s55, s55, 1024\n\t"
    "s_add_u32 s56, s56, 32768\n\t"
    "s_mov_b32 m0, s55\n\t"
    "s_nop 0\n\t"
    "buffer_load_dwordx4 v26, s[40:43], s56 offen lds\n\t"
    "s_add_u32 s55, s55, 1024\n\t"
    "s_add_u32 s56, s56, 32768\n\t"
    "s_mov_b32 m0, s55\n\t"
    "s_nop 0\n\t"
    "buffer_load_dwordx4 v26, s[40:43], s56 offen lds\n\t"
    "s_add_u32 s55, s55, 1024\n\t"
    "s_add_u32 s56, s56, 32768\n\t"
    "s_mov_b32 m0, s55\n\t"
    "s_nop 0\n\t"
    "buffer_load_dwordx4 v26, s[40:43], s56 offen lds\n\t"
    "s_add_u32 s55, s55, 1024\n\t"
    "s_add_u32 s56, s56, 32768\n\t"
    "s_mov_b32 m0, s55\n\t"
    "s_nop 0\n\t"
    "buffer_load_dwordx4 v26, s[40:43], s56 offen lds\n\t"
    "s_add_u32 s55, s55, 1024\n\t"
    "s_add_u32 s56, s56, 32768\n\t"
    "s_mov_b32 m0, s55\n\t"
    "s_nop 0\n\t"
    "buffer_load_dwordx4 v26, s[40:43], s56 offen lds\n\t"
    "s_waitcnt vmcnt(16)\n\t"
    "s_barrier\n\t"
    "s_lshr_b32 s55, s51, 1\n\t"
    "s_lshl_b32 s55, s55, 15\n\t"
    "s_and_b32 s56, s51, 1\n\t"
    "s_lshl_b32 s56, s56, 7\n\t"
    "s_add_u32 s55, s55, s56\n\t"
    "s_add_u32 s55, s55, 16384\n\t"
    "v_lshlrev_b32 v28, 8, v27\n\t"
    "v_lshl_add_u32 v28, v17, 3, v28\n\t"
    "v_add_u32 v28, s55, v28\n\t"
    "ds_read_b64 v[32:33], v28 offset:0\n\t"
    "ds_read_b64 v[34:35], v28 offset:256\n\t"
    "ds_read_b64 v[36:37], v28 offset:8192\n\t"
    "ds_read_b64 v[38:39], v28 offset:8448\n\t"
    "ds_read_b64 v[40:41], v28 offset:2048\n\t"
    "ds_read_b64 v[42:43], v28 offset:2304\n\t"
    "ds_read_b64 v[44:45], v28 offset:10240\n\t"
    "ds_read_b64 v[46:47], v28 offset:10496\n\t"
    "ds_read_b64 v[48:49], v28 offset:512\n\t"
    "ds_read_b64 v[50:51], v28 offset:768\n\t"
    "ds_read_b64 v[52:53], v28 offset:8704\n\t"
    "ds_read_b64 v[54:55], v28 offset:8960\n\t"
    "ds_read_b64 v[56:57], v28 offset:2560\n\t"
    "ds_read_b64 v[58:59], v28 offset:2816\n\t"
    "ds_read_b64 v[60:61], v28 offset:10752\n\t"
    "ds_read_b64 v[62:63], v28 offset:11008\n\t"
    "s_waitcnt lgkmcnt(14)\n\t"
    "v_perm_b32 v29, v34, v32, s61\n\t"
    "v_accvgpr_write_b32 a16, v29\n\t"
    "s_waitcnt lgkmcnt(12)\n\t"
    "v_perm_b32 v29, v38, v36, s61\n\t"
    "v_accvgpr_write_b32 a17, v29\n\t"
    "s_waitcnt lgkmcnt(10)\n\t"
    "v_perm_b32 v29, v42, v40, s61\n\t"
    "v_accvgpr_write_b32 a18, v29\n\t"
    "s_waitcnt lgkmcnt(8)\n\t"
    "v_perm_b32 v29, v46, v44, s61\n\t"
    "v_accvgpr_write_b32 a19, v29\n\t"
    "s_waitcnt lgkmcnt(6)\n\t"
    "v_perm_b32 v29, v50, v48, s61\n\t"
    "v_accvgpr_write_b32 a20, v29\n\t"
    "s_waitcnt lgkmcnt(4)\n\t"
    "v_perm_b32 v29, v54, v52, s61\n\t"
    "v_accvgpr_write_b32 a21, v29\n\t"
    "s_waitcnt lgkmcnt(2)\n\t"
    "v_perm_b32 v29, v58, v56, s61\n\t"
    "v_accvgpr_write_b32 a22, v29\n\t"
    "s_waitcnt lgkmcnt(0)\n\t"
    "v_perm_b32 v29, v62, v60, s61\n\t"
    "v_accvgpr_write_b32 a23, v29\n\t"
    "v_perm_b32 v29, v34, v32, s62\n\t"
    "v_accvgpr_write_b32 a80, v29\n\t"
    "v_perm_b32 v29, v38, v36, s62\n\t"
    "v_accvgpr_write_b32 a81, v29\n\t"
    "v_perm_b32 v29, v42, v40, s62\n\t"
    "v_accvgpr_write_b32 a82, v29\n\t"
    "v_perm_b32 v29, v46, v44, s62\n\t"
    "v_accvgpr_write_b32 a83, v29\n\t"
    "v_perm_b32 v29, v50, v48, s62\n\t"
    "v_accvgpr_write_b32 a84, v29\n\t"
    "v_perm_b32 v29, v54, v52, s62\n\t"
    "v_accvgpr_write_b32 a85, v29\n\t"
    "v_perm_b32 v29, v58, v56, s62\n\t"
    "v_accvgpr_write_b32 a86, v29\n\t"
    "v_perm_b32 v29, v62, v60, s62\n\t"
    "v_accvgpr_write_b32 a87, v29\n\t"
    "v_perm_b32 v29, v35, v33, s61\n\t"
    "v_accvgpr_write_b32 a144, v29\n\t"
    "v_perm_b32 v29, v39, v37, s61\n\t"
    "v_accvgpr_write_b32 a145, v29\n\t"
    "v_perm_b32 v29, v43, v41, s61\n\t"
    "v_accvgpr_write_b32 a146, v29\n\t"
    "v_perm_b32 v29, v47, v45, s61\n\t"
    "v_accvgpr_write_b32 a147, v29\n\t"
    "v_perm_b32 v29, v51, v49, s61\n\t"
    "v_accvgpr_write_b32 a148, v29\n\t"
    "v_perm_b32 v29, v55, v53, s61\n\t"
    "v_accvgpr_write_b32 a149, v29\n\t"
    "v_perm_b32 v29, v59, v57, s61\n\t"
    "v_accvgpr_write_b32 a150, v29\n\t"
    "v_perm_b32 v29, v63, v61, s61\n\t"
    "v_accvgpr_write_b32 a151, v29\n\t"
    "v_perm_b32 v80, v35, v33, s62\n\t"
    "v_perm_b32 v81, v39, v37, s62\n\t"
    "v_perm_b32 v82, v43, v41, s62\n\t"
    "v_perm_b32 v83, v47, v45, s62\n\t"
    "v_perm_b32 v84, v51, v49, s62\n\t"
    "v_perm_b32 v85, v55, v53, s62\n\t"
    "v_perm_b32 v86, v59, v57, s62\n\t"
    "v_perm_b32 v87, v63, v61, s62\n\t"
    "ds_read_b64 v[32:33], v28 offset:1024\n\t"
    "ds_read_b64 v[34:35], v28 offset:1280\n\t"
    "ds_read_b64 v[36:37], v28 offset:9216\n\t"
    "ds_read_b64 v[38:39], v28 offset:9472\n\t"
    "ds_read_b64 v[40:41], v28 offset:3072\n\t"
    "ds_read_b64 v[42:43], v28 offset:3328\n\t"
    "ds_read_b64 v[44:45], v28 offset:11264\n\t"
    "ds_read_b64 v[46:47], v28 offset:11520\n\t"
    "ds_read_b64 v[48:49], v28 offset:1536\n\t"
    "ds_read_b64 v[50:51], v28 offset:1792\n\t"
    "ds_read_b64 v[52:53], v28 offset:9728\n\t"
    "ds_read_b64 v[54:55], v28 offset:9984\n\t"
    "ds_read_b64 v[56:57], v28 offset:3584\n\t"
    "ds_read_b64 v[58:59], v28 offset:3840\n\t"
    "ds_read_b64 v[60:61], v28 offset:11776\n\t"
    "ds_read_b64 v[62:63], v28 offset:12032\n\t"
    "s_waitcnt lgkmcnt(14)\n\t"
    "v_perm_b32 v29, v34, v32, s61\n\t"
    "v_accvgpr_write_b32 a24, v29\n\t"
    "s_waitcnt lgkmcnt(12)\n\t"
    "v_perm_b32 v29, v38, v36, s61\n\t"
    "v_accvgpr_write_b32 a25, v29\n\t"
    "s_waitcnt lgkmcnt(10)\n\t"
    "v_perm_b32 v29, v42, v40, s61\n\t"
    "v_accvgpr_write_b32 a26, v29\n\t"
    "s_waitcnt lgkmcnt(8)\n\t"
    "v_perm_b32 v29, v46, v44, s61\n\t"
    "v_accvgpr_write_b32 a27, v29\n\t"
    "s_waitcnt lgkmcnt(6)\n\t"
    "v_perm_b32 v29, v50, v48, s61\n\t"
    "v_accvgpr_write_b32 a28, v29\n\t"
    "s_waitcnt lgkmcnt(4)\n\t"
    "v_perm_b32 v29, v54, v52, s61\n\t"
    "v_accvgpr_write_b32 a29, v29\n\t"
    "s_waitcnt lgkmcnt(2)\n\t"
    "v_perm_b32 v29, v58, v56, s61\n\t"
    "v_accvgpr_write_b32 a30, v29\n\t"
    "s_waitcnt lgkmcnt(0)\n\t"
    "v_perm_b32 v29, v62, v60, s61\n\t"
    "v_accvgpr_write_b32 a31, v29\n\t"
    "v_perm_b32 v29, v34, v32, s62\n\t"
    "v_accvgpr_write_b32 a88, v29\n\t"
    "v_perm_b32 v29, v38, v36, s62\n\t"
    "v_accvgpr_write_b32 a89, v29\n\t"
    "v_perm_b32 v29, v42, v40, s62\n\t"
    "v_accvgpr_write_b32 a90, v29\n\t"
    "v_perm_b32 v29, v46, v44, s62\n\t"
    "v_accvgpr_write_b32 a91, v29\n\t"
    "v_perm_b32 v29, v50, v48, s62\n\t"
    "v_accvgpr_write_b32 a92, v29\n\t"
    "v_perm_b32 v29, v54, v52, s62\n\t"
    "v_accvgpr_write_b32 a93, v29\n\t"
    "v_perm_b32 v29, v58, v56, s62\n\t"
    "v_accvgpr_write_b32 a94, v29\n\t"
    "v_perm_b32 v29, v62, v60, s62\n\t"
    "v_accvgpr_write_b32 a95, v29\n\t"
    "v_perm_b32 v29, v35, v33, s61\n\t"
    "v_accvgpr_write_b32 a152, v29\n\t"
    "v_perm_b32 v29, v39, v37, s61\n\t"
    "v_accvgpr_write_b32 a153, v29\n\t"
    "v_perm_b32 v29, v43, v41, s61\n\t"
    "v_accvgpr_write_b32 a154, v29\n\t"
    "v_perm_b32 v29, v47, v45, s61\n\t"
    "v_accvgpr_write_b32 a155, v29\n\t"
    "v_perm_b32 v29, v51, v49, s61\n\t"
    "v_accvgpr_write_b32 a156, v29\n\t"
    "v_perm_b32 v29, v55, v53, s61\n\t"
    "v_accvgpr_write_b32 a157, v29\n\t"
    "v_perm_b32 v29, v59, v57, s61\n\t"
    "v_accvgpr_write_b32 a158, v29\n\t"
    "v_perm_b32 v29, v63, v61, s61\n\t"
    "v_accvgpr_write_b32 a159, v29\n\t"
    "v_perm_b32 v88, v35, v33, s62\n\t"
    "v_perm_b32 v89, v39, v37, s62\n\t"
    "v_perm_b32 v90, v43, v41, s62\n\t"
    "v_perm_b32 v91, v47, v45, s62\n\t"
    "v_perm_b32 v92, v51, v49, s62\n\t"
    "v_perm_b32 v93, v55, v53, s62\n\t"
    "v_perm_b32 v94, v59, v57, s62\n\t"
    "v_perm_b32 v95, v63, v61, s62\n\t"
    "s_waitcnt lgkmcnt(0)\n\t"
    "s_barrier\n\t"
    "s_lshl_b32 s55, s51, 14\n\t"
    "s_add_u32 s55, s55, 16384\n\t"
    "s_cmp_lt_u32 s51, 2\n\t"
    "s_cselect_b32 s56, %[wq_lo], %[wk_lo]\n\t"
    "s_cselect_b32 s57, %[wq_hi], %[wk_hi]\n\t"
    "s_lshl_b32 s65, s52, 8\n\t"
    "s_mov_b32 s40, s56\n\t"
    "s_mov_b32 s41, s57\n\t"
    "s_add_u32 s40, s40, s65\n\t"
    "s_addc_u32 s41, s41, 0\n\t"
    "s_sub_u32 s42, 4194304, s65\n\t"
    "s_and_b32 s41, s41, 0xffff\n\t"
    "s_and_b32 s56, s51, 1\n\t"
    "s_lshl_b32 s56, s56, 19\n\t"
    "s_add_u32 s56, s56, 3145728\n\t"
    "s_mov_b32 m0, s55\n\t"
    "s_nop 0\n\t"
    "buffer_load_dwordx4 v26, s[40:43], s56 offen lds\n\t"
    "s_add_u32 s55, s55, 1024\n\t"
    "s_add_u32 s56, s56, 32768\n\t"
    "s_mov_b32 m0, s55\n\t"
    "s_nop 0\n\t"
    "buffer_load_dwordx4 v26, s[40:43], s56 offen lds\n\t"
    "s_add_u32 s55, s55, 1024\n\t"
    "s_add_u32 s56, s56, 32768\n\t"
    "s_mov_b32 m0, s55\n\t"
    "s_nop 0\n\t"
    "buffer_load_dwordx4 v26, s[40:43], s56 offen lds\n\t"
    "s_add_u32 s55, s55, 1024\n\t"
    "s_add_u32 s56, s56, 32768\n\t"
    "s_mov_b32 m0, s55\n\t"
    "s_nop 0\n\t"
    "buffer_load_dwordx4 v26, s[40:43], s56 offen lds\n\t"
    "s_add_u32 s55, s55, 1024\n\t"
    "s_add_u32 s56, s56, 32768\n\t"
    "s_mov_b32 m0, s55\n\t"
    "s_nop 0\n\t"
    "buffer_load_dwordx4 v26, s[40:43], s56 offen lds\n\t"
    "s_add_u32 s55, s55, 1024\n\t"
    "s_add_u32 s56, s56, 32768\n\t"
    "s_mov_b32 m0, s55\n\t"
    "s_nop 0\n\t"
    "buffer_load_dwordx4 v26, s[40:43], s56 offen lds\n\t"
    "s_add_u32 s55, s55, 1024\n\t"
    "s_add_u32 s56, s56, 32768\n\t"
    "s_mov_b32 m0, s55\n\t"
    "s_nop 0\n\t"
    "buffer_load_dwordx4 v26, s[40:43], s56 offen lds\n\t"
    "s_add_u32 s55, s55, 1024\n\t"
    "s_add_u32 s56, s56, 32768\n\t"
    "s_mov_b32 m0, s55\n\t"
    "s_nop 0\n\t"
    "buffer_load_dwordx4 v26, s[40:43], s56 offen lds\n\t"
    "s_add_u32 s55, s55, 1024\n\t"
    "s_add_u32 s56, s56, 32768\n\t"
    "s_mov_b32 m0, s55\n\t"
    "s_nop 0\n\t"
    "buffer_load_dwordx4 v26, s[40:43], s56 offen lds\n\t"
    "s_add_u32 s55, s55, 1024\n\t"
    "s_add_u32 s56, s56, 32768\n\t"
    "s_mov_b32 m0, s55\n\t"
    "s_nop 0\n\t"
    "buffer_load_dwordx4 v26, s[40:43], s56 offen lds\n\t"
    "s_add_u32 s55, s55, 1024\n\t"
    "s_add_u32 s56, s56, 32768\n\t"
    "s_mov_b32 m0, s55\n\t"
    "s_nop 0\n\t"
    "buffer_load_dwordx4 v26, s[40:43], s56 offen lds\n\t"
    "s_add_u32 s55, s55, 1024\n\t"
    "s_add_u32 s56, s56, 32768\n\t"
    "s_mov_b32 m0, s55\n\t"
    "s_nop 0\n\t"
    "buffer_load_dwordx4 v26, s[40:43], s56 offen lds\n\t"
    "s_add_u32 s55, s55, 1024\n\t"
    "s_add_u32 s56, s56, 32768\n\t"
    "s_mov_b32 m0, s55\n\t"
    "s_nop 0\n\t"
    "buffer_load_dwordx4 v26, s[40:43], s56 offen lds\n\t"
    "s_add_u32 s55, s55, 1024\n\t"
    "s_add_u32 s56, s56, 32768\n\t"
    "s_mov_b32 m0, s55\n\t"
    "s_nop 0\n\t"
    "buffer_load_dwordx4 v26, s[40:43], s56 offen lds\n\t"
    "s_add_u32 s55, s55, 1024\n\t"
    "s_add_u32 s56, s56, 32768\n\t"
    "s_mov_b32 m0, s55\n\t"
    "s_nop 0\n\t"
    "buffer_load_dwordx4 v26, s[40:43], s56 offen lds\n\t"
    "s_add_u32 s55, s55, 1024\n\t"
    "s_add_u32 s56, s56, 32768\n\t"
    "s_mov_b32 m0, s55\n\t"
    "s_nop 0\n\t"
    "buffer_load_dwordx4 v26, s[40:43], s56 offen lds\n\t"
    "s_waitcnt vmcnt(16)\n\t"
    "s_barrier\n\t"
    "s_lshr_b32 s55, s51, 1\n\t"
    "s_lshl_b32 s55, s55, 15\n\t"
    "s_and_b32 s56, s51, 1\n\t"
    "s_lshl_b32 s56, s56, 7\n\t"
    "s_add_u32 s55, s55, s56\n\t"
    "s_add_u32 s55, s55, 81920\n\t"
    "v_lshlrev_b32 v28, 8, v27\n\t"
    "v_lshl_add_u32 v28, v17, 3, v28\n\t"
    "v_add_u32 v28, s55, v28\n\t"
    "ds_read_b64 v[32:33], v28 offset:0\n\t"
    "ds_read_b64 v[34:35], v28 offset:256\n\t"
    "ds_read_b64 v[36:37], v28 offset:8192\n\t"
    "ds_read_b64 v[38:39], v28 offset:8448\n\t"
    "ds_read_b64 v[40:41], v28 offset:2048\n\t"
    "ds_read_b64 v[42:43], v28 offset:2304\n\t"
    "ds_read_b64 v[44:45], v28 offset:10240\n\t"
    "ds_read_b64 v[46:47], v28 offset:10496\n\t"
    "ds_read_b64 v[48:49], v28 offset:512\n\t"
    "ds_read_b64 v[50:51], v28 offset:768\n\t"
    "ds_read_b64 v[52:53], v28 offset:8704\n\t"
    "ds_read_b64 v[54:55], v28 offset:8960\n\t"
    "ds_read_b64 v[56:57], v28 offset:2560\n\t"
    "ds_read_b64 v[58:59], v28 offset:2816\n\t"
    "ds_read_b64 v[60:61], v28 offset:10752\n\t"
    "ds_read_b64 v[62:63], v28 offset:11008\n\t"
    "s_waitcnt lgkmcnt(14)\n\t"
    "v_perm_b32 v29, v34, v32, s61\n\t"
    "v_accvgpr_write_b32 a32, v29\n\t"
    "s_waitcnt lgkmcnt(12)\n\t"
    "v_perm_b32 v29, v38, v36, s61\n\t"
    "v_accvgpr_write_b32 a33, v29\n\t"
    "s_waitcnt lgkmcnt(10)\n\t"
    "v_perm_b32 v29, v42, v40, s61\n\t"
    "v_accvgpr_write_b32 a34, v29\n\t"
    "s_waitcnt lgkmcnt(8)\n\t"
    "v_perm_b32 v29, v46, v44, s61\n\t"
    "v_accvgpr_write_b32 a35, v29\n\t"
    "s_waitcnt lgkmcnt(6)\n\t"
    "v_perm_b32 v29, v50, v48, s61\n\t"
    "v_accvgpr_write_b32 a36, v29\n\t"
    "s_waitcnt lgkmcnt(4)\n\t"
    "v_perm_b32 v29, v54, v52, s61\n\t"
    "v_accvgpr_write_b32 a37, v29\n\t"
    "s_waitcnt lgkmcnt(2)\n\t"
    "v_perm_b32 v29, v58, v56, s61\n\t"
    "v_accvgpr_write_b32 a38, v29\n\t"
    "s_waitcnt lgkmcnt(0)\n\t"
    "v_perm_b32 v29, v62, v60, s61\n\t"
    "v_accvgpr_write_b32 a39, v29\n\t"
    "v_perm_b32 v29, v34, v32, s62\n\t"
    "v_accvgpr_write_b32 a96, v29\n\t"
    "v_perm_b32 v29, v38, v36, s62\n\t"
    "v_accvgpr_write_b32 a97, v29\n\t"
    "v_perm_b32 v29, v42, v40, s62\n\t"
    "v_accvgpr_write_b32 a98, v29\n\t"
    "v_perm_b32 v29, v46, v44, s62\n\t"
    "v_accvgpr_write_b32 a99, v29\n\t"
    "v_perm_b32 v29, v50, v48, s62\n\t"
    "v_accvgpr_write_b32 a100, v29\n\t"
    "v_perm_b32 v29, v54, v52, s62\n\t"
    "v_accvgpr_write_b32 a101, v29\n\t"
    "v_perm_b32 v29, v58, v56, s62\n\t"
    "v_accvgpr_write_b32 a102, v29\n\t"
    "v_perm_b32 v29, v62, v60, s62\n\t"
    "v_accvgpr_write_b32 a103, v29\n\t"
    "v_perm_b32 v29, v35, v33, s61\n\t"
    "v_accvgpr_write_b32 a160, v29\n\t"
    "v_perm_b32 v29, v39, v37, s61\n\t"
    "v_accvgpr_write_b32 a161, v29\n\t"
    "v_perm_b32 v29, v43, v41, s61\n\t"
    "v_accvgpr_write_b32 a162, v29\n\t"
    "v_perm_b32 v29, v47, v45, s61\n\t"
    "v_accvgpr_write_b32 a163, v29\n\t"
    "v_perm_b32 v29, v51, v49, s61\n\t"
    "v_accvgpr_write_b32 a164, v29\n\t"
    "v_perm_b32 v29, v55, v53, s61\n\t"
    "v_accvgpr_write_b32 a165, v29\n\t"
    "v_perm_b32 v29, v59, v57, s61\n\t"
    "v_accvgpr_write_b32 a166, v29\n\t"
    "v_perm_b32 v29, v63, v61, s61\n\t"
    "v_accvgpr_write_b32 a167, v29\n\t"
    "v_perm_b32 v96, v35, v33, s62\n\t"
    "v_perm_b32 v97, v39, v37, s62\n\t"
    "v_perm_b32 v98, v43, v41, s62\n\t"
    "v_perm_b32 v99, v47, v45, s62\n\t"
    "v_perm_b32 v100, v51, v49, s62\n\t"
    "v_perm_b32 v101, v55, v53, s62\n\t"
    "v_perm_b32 v102, v59, v57, s62\n\t"
    "v_perm_b32 v103, v63, v61, s62\n\t"
    "ds_read_b64 v[32:33], v28 offset:1024\n\t"
    "ds_read_b64 v[34:35], v28 offset:1280\n\t"
    "ds_read_b64 v[36:37], v28 offset:9216\n\t"
    "ds_read_b64 v[38:39], v28 offset:9472\n\t"
    "ds_read_b64 v[40:41], v28 offset:3072\n\t"
    "ds_read_b64 v[42:43], v28 offset:3328\n\t"
    "ds_read_b64 v[44:45], v28 offset:11264\n\t"
    "ds_read_b64 v[46:47], v28 offset:11520\n\t"
    "ds_read_b64 v[48:49], v28 offset:1536\n\t"
    "ds_read_b64 v[50:51], v28 offset:1792\n\t"
    "ds_read_b64 v[52:53], v28 offset:9728\n\t"
    "ds_read_b64 v[54:55], v28 offset:9984\n\t"
    "ds_read_b64 v[56:57], v28 offset:3584\n\t"
    "ds_read_b64 v[58:59], v28 offset:3840\n\t"
    "ds_read_b64 v[60:61], v28 offset:11776\n\t"
    "ds_read_b64 v[62:63], v28 offset:12032\n\t"
    "s_waitcnt lgkmcnt(14)\n\t"
    "v_perm_b32 v29, v34, v32, s61\n\t"
    "v_accvgpr_write_b32 a40, v29\n\t"
    "s_waitcnt lgkmcnt(12)\n\t"
    "v_perm_b32 v29, v38, v36, s61\n\t"
    "v_accvgpr_write_b32 a41, v29\n\t"
    "s_waitcnt lgkmcnt(10)\n\t"
    "v_perm_b32 v29, v42, v40, s61\n\t"
    "v_accvgpr_write_b32 a42, v29\n\t"
    "s_waitcnt lgkmcnt(8)\n\t"
    "v_perm_b32 v29, v46, v44, s61\n\t"
    "v_accvgpr_write_b32 a43, v29\n\t"
    "s_waitcnt lgkmcnt(6)\n\t"
    "v_perm_b32 v29, v50, v48, s61\n\t"
    "v_accvgpr_write_b32 a44, v29\n\t"
    "s_waitcnt lgkmcnt(4)\n\t"
    "v_perm_b32 v29, v54, v52, s61\n\t"
    "v_accvgpr_write_b32 a45, v29\n\t"
    "s_waitcnt lgkmcnt(2)\n\t"
    "v_perm_b32 v29, v58, v56, s61\n\t"
    "v_accvgpr_write_b32 a46, v29\n\t"
    "s_waitcnt lgkmcnt(0)\n\t"
    "v_perm_b32 v29, v62, v60, s61\n\t"
    "v_accvgpr_write_b32 a47, v29\n\t"
    "v_perm_b32 v29, v34, v32, s62\n\t"
    "v_accvgpr_write_b32 a104, v29\n\t"
    "v_perm_b32 v29, v38, v36, s62\n\t"
    "v_accvgpr_write_b32 a105, v29\n\t"
    "v_perm_b32 v29, v42, v40, s62\n\t"
    "v_accvgpr_write_b32 a106, v29\n\t"
    "v_perm_b32 v29, v46, v44, s62\n\t"
    "v_accvgpr_write_b32 a107, v29\n\t"
    "v_perm_b32 v29, v50, v48, s62\n\t"
    "v_accvgpr_write_b32 a108, v29\n\t"
    "v_perm_b32 v29, v54, v52, s62\n\t"
    "v_accvgpr_write_b32 a109, v29\n\t"
    "v_perm_b32 v29, v58, v56, s62\n\t"
    "v_accvgpr_write_b32 a110, v29\n\t"
    "v_perm_b32 v29, v62, v60, s62\n\t"
    "v_accvgpr_write_b32 a111, v29\n\t"
    "v_perm_b32 v29, v35, v33, s61\n\t"
    "v_accvgpr_write_b32 a168, v29\n\t"
    "v_perm_b32 v29, v39, v37, s61\n\t"
    "v_accvgpr_write_b32 a169, v29\n\t"
    "v_perm_b32 v29, v43, v41, s61\n\t"
    "v_accvgpr_write_b32 a170, v29\n\t"
    "v_perm_b32 v29, v47, v45, s61\n\t"
    "v_accvgpr_write_b32 a171, v29\n\t"
    "v_perm_b32 v29, v51, v49, s61\n\t"
    "v_accvgpr_write_b32 a172, v29\n\t"
    "v_perm_b32 v29, v55, v53, s61\n\t"
    "v_accvgpr_write_b32 a173, v29\n\t"
    "v_perm_b32 v29, v59, v57, s61\n\t"
    "v_accvgpr_write_b32 a174, v29\n\t"
    "v_perm_b32 v29, v63, v61, s61\n\t"
    "v_accvgpr_write_b32 a175, v29\n\t"
    "v_perm_b32 v104, v35, v33, s62\n\t"
    "v_perm_b32 v105, v39, v37, s62\n\t"
    "v_perm_b32 v106, v43, v41, s62\n\t"
    "v_perm_b32 v107, v47, v45, s62\n\t"
    "v_perm_b32 v108, v51, v49, s62\n\t"
    "v_perm_b32 v109, v55, v53, s62\n\t"
    "v_perm_b32 v110, v59, v57, s62\n\t"
    "v_perm_b32 v111, v63, v61, s62\n\t"
    "s_waitcnt lgkmcnt(0)\n\t"
    "s_barrier\n\t"
    "s_lshl_b32 s55, s51, 14\n\t"
    "s_add_u32 s55, s55, 81920\n\t"
    "s_lshl_b32 s56, s52, 8\n\t"
    "s_mov_b32 s40, %[wv_lo]\n\t"
    "s_mov_b32 s41, %[wv_hi]\n\t"
    "s_add_u32 s40, s40, s56\n\t"
    "s_addc_u32 s41, s41, 0\n\t"
    "s_sub_u32 s42, 4194304, s56\n\t"
    "s_and_b32 s41, s41, 0xffff\n\t"
    "s_lshl_b32 s56, s51, 19\n\t"
    "s_add_u32 s56, s56, 2097152\n\t"
    "s_mov_b32 m0, s55\n\t"
    "s_nop 0\n\t"
    "buffer_load_dwordx4 v26, s[40:43], s56 offen lds\n\t"
    "s_add_u32 s55, s55, 1024\n\t"
    "s_add_u32 s56, s56, 32768\n\t"
    "s_mov_b32 m0, s55\n\t"
    "s_nop 0\n\t"
    "buffer_load_dwordx4 v26, s[40:43], s56 offen lds\n\t"
    "s_add_u32 s55, s55, 1024\n\t"
    "s_add_u32 s56, s56, 32768\n\t"
    "s_mov_b32 m0, s55\n\t"
    "s_nop 0\n\t"
    "buffer_load_dwordx4 v26, s[40:43], s56 offen lds\n\t"
    "s_add_u32 s55, s55, 1024\n\t"
    "s_add_u32 s56, s56, 32768\n\t"
    "s_mov_b32 m0, s55\n\t"
    "s_nop 0\n\t"
    "buffer_load_dwordx4 v26, s[40:43], s56 offen lds\n\t"
    "s_add_u32 s55, s55, 1024\n\t"
    "s_add_u32 s56, s56, 32768\n\t"
    "s_mov_b32 m0, s55\n\t"
    "s_nop 0\n\t"
    "buffer_load_dwordx4 v26, s[40:43], s56 offen lds\n\t"
    "s_add_u32 s55, s55, 1024\n\t"
    "s_add_u32 s56, s56, 32768\n\t"
    "s_mov_b32 m0, s55\n\t"
    "s_nop 0\n\t"
    "buffer_load_dwordx4 v26, s[40:43], s56 offen lds\n\t"
    "s_add_u32 s55, s55, 1024\n\t"
    "s_add_u32 s56, s56, 32768\n\t"
    "s_mov_b32 m0, s55\n\t"
    "s_nop 0\n\t"
    "buffer_load_dwordx4 v26, s[40:43], s56 offen lds\n\t"
    "s_add_u32 s55, s55, 1024\n\t"
    "s_add_u32 s56, s56, 32768\n\t"
    "s_mov_b32 m0, s55\n\t"
    "s_nop 0\n\t"
    "buffer_load_dwordx4 v26, s[40:43], s56 offen lds\n\t"
    "s_add_u32 s55, s55, 1024\n\t"
    "s_add_u32 s56, s56, 32768\n\t"
    "s_mov_b32 m0, s55\n\t"
    "s_nop 0\n\t"
    "buffer_load_dwordx4 v26, s[40:43], s56 offen lds\n\t"
    "s_add_u32 s55, s55, 1024\n\t"
    "s_add_u32 s56, s56, 32768\n\t"
    "s_mov_b32 m0, s55\n\t"
    "s_nop 0\n\t"
    "buffer_load_dwordx4 v26, s[40:43], s56 offen lds\n\t"
    "s_add_u32 s55, s55, 1024\n\t"
    "s_add_u32 s56, s56, 32768\n\t"
    "s_mov_b32 m0, s55\n\t"
    "s_nop 0\n\t"
    "buffer_load_dwordx4 v26, s[40:43], s56 offen lds\n\t"
    "s_add_u32 s55, s55, 1024\n\t"
    "s_add_u32 s56, s56, 32768\n\t"
    "s_mov_b32 m0, s55\n\t"
    "s_nop 0\n\t"
    "buffer_load_dwordx4 v26, s[40:43], s56 offen lds\n\t"
    "s_add_u32 s55, s55, 1024\n\t"
    "s_add_u32 s56, s56, 32768\n\t"
    "s_mov_b32 m0, s55\n\t"
    "s_nop 0\n\t"
    "buffer_load_dwordx4 v26, s[40:43], s56 offen lds\n\t"
    "s_add_u32 s55, s55, 1024\n\t"
    "s_add_u32 s56, s56, 32768\n\t"
    "s_mov_b32 m0, s55\n\t"
    "s_nop 0\n\t"
    "buffer_load_dwordx4 v26, s[40:43], s56 offen lds\n\t"
    "s_add_u32 s55, s55, 1024\n\t"
    "s_add_u32 s56, s56, 32768\n\t"
    "s_mov_b32 m0, s55\n\t"
    "s_nop 0\n\t"
    "buffer_load_dwordx4 v26, s[40:43], s56 offen lds\n\t"
    "s_add_u32 s55, s55, 1024\n\t"
    "s_add_u32 s56, s56, 32768\n\t"
    "s_mov_b32 m0, s55\n\t"
    "s_nop 0\n\t"
    "buffer_load_dwordx4 v26, s[40:43], s56 offen lds\n\t"
    "s_waitcnt vmcnt(16)\n\t"
    "s_barrier\n\t"
    "s_lshr_b32 s55, s51, 1\n\t"
    "s_lshl_b32 s55, s55, 15\n\t"
    "s_and_b32 s56, s51, 1\n\t"
    "s_lshl_b32 s56, s56, 7\n\t"
    "s_add_u32 s55, s55, s56\n\t"
    "s_add_u32 s55, s55, 16384\n\t"
    "v_lshlrev_b32 v28, 8, v27\n\t"
    "v_lshl_add_u32 v28, v17, 3, v28\n\t"
    "v_add_u32 v28, s55, v28\n\t"
    "ds_read_b64 v[32:33], v28 offset:0\n\t"
    "ds_read_b64 v[34:35], v28 offset:256\n\t"
    "ds_read_b64 v[36:37], v28 offset:8192\n\t"
    "ds_read_b64 v[38:39], v28 offset:8448\n\t"
    "ds_read_b64 v[40:41], v28 offset:2048\n\t"
    "ds_read_b64 v[42:43], v28 offset:2304\n\t"
    "ds_read_b64 v[44:45], v28 offset:10240\n\t"
    "ds_read_b64 v[46:47], v28 offset:10496\n\t"
    "ds_read_b64 v[48:49], v28 offset:512\n\t"
    "ds_read_b64 v[50:51], v28 offset:768\n\t"
    "ds_read_b64 v[52:53], v28 offset:8704\n\t"
    "ds_read_b64 v[54:55], v28 offset:8960\n\t"
    "ds_read_b64 v[56:57], v28 offset:2560\n\t"
    "ds_read_b64 v[58:59], v28 offset:2816\n\t"
    "ds_read_b64 v[60:61], v28 offset:10752\n\t"
    "ds_read_b64 v[62:63], v28 offset:11008\n\t"
    "s_waitcnt lgkmcnt(14)\n\t"
    "v_perm_b32 v29, v34, v32, s61\n\t"
    "v_accvgpr_write_b32 a48, v29\n\t"
    "s_waitcnt lgkmcnt(12)\n\t"
    "v_perm_b32 v29, v38, v36, s61\n\t"
    "v_accvgpr_write_b32 a49, v29\n\t"
    "s_waitcnt lgkmcnt(10)\n\t"
    "v_perm_b32 v29, v42, v40, s61\n\t"
    "v_accvgpr_write_b32 a50, v29\n\t"
    "s_waitcnt lgkmcnt(8)\n\t"
    "v_perm_b32 v29, v46, v44, s61\n\t"
    "v_accvgpr_write_b32 a51, v29\n\t"
    "s_waitcnt lgkmcnt(6)\n\t"
    "v_perm_b32 v29, v50, v48, s61\n\t"
    "v_accvgpr_write_b32 a52, v29\n\t"
    "s_waitcnt lgkmcnt(4)\n\t"
    "v_perm_b32 v29, v54, v52, s61\n\t"
    "v_accvgpr_write_b32 a53, v29\n\t"
    "s_waitcnt lgkmcnt(2)\n\t"
    "v_perm_b32 v29, v58, v56, s61\n\t"
    "v_accvgpr_write_b32 a54, v29\n\t"
    "s_waitcnt lgkmcnt(0)\n\t"
    "v_perm_b32 v29, v62, v60, s61\n\t"
    "v_accvgpr_write_b32 a55, v29\n\t"
    "v_perm_b32 v29, v34, v32, s62\n\t"
    "v_accvgpr_write_b32 a112, v29\n\t"
    "v_perm_b32 v29, v38, v36, s62\n\t"
    "v_accvgpr_write_b32 a113, v29\n\t"
    "v_perm_b32 v29, v42, v40, s62\n\t"
    "v_accvgpr_write_b32 a114, v29\n\t"
    "v_perm_b32 v29, v46, v44, s62\n\t"
    "v_accvgpr_write_b32 a115, v29\n\t"
    "v_perm_b32 v29, v50, v48, s62\n\t"
    "v_accvgpr_write_b32 a116, v29\n\t"
    "v_perm_b32 v29, v54, v52, s62\n\t"
    "v_accvgpr_write_b32 a117, v29\n\t"
    "v_perm_b32 v29, v58, v56, s62\n\t"
    "v_accvgpr_write_b32 a118, v29\n\t"
    "v_perm_b32 v29, v62, v60, s62\n\t"
    "v_accvgpr_write_b32 a119, v29\n\t"
    "v_perm_b32 v29, v35, v33, s61\n\t"
    "v_accvgpr_write_b32 a176, v29\n\t"
    "v_perm_b32 v29, v39, v37, s61\n\t"
    "v_accvgpr_write_b32 a177, v29\n\t"
    "v_perm_b32 v29, v43, v41, s61\n\t"
    "v_accvgpr_write_b32 a178, v29\n\t"
    "v_perm_b32 v29, v47, v45, s61\n\t"
    "v_accvgpr_write_b32 a179, v29\n\t"
    "v_perm_b32 v29, v51, v49, s61\n\t"
    "v_accvgpr_write_b32 a180, v29\n\t"
    "v_perm_b32 v29, v55, v53, s61\n\t"
    "v_accvgpr_write_b32 a181, v29\n\t"
    "v_perm_b32 v29, v59, v57, s61\n\t"
    "v_accvgpr_write_b32 a182, v29\n\t"
    "v_perm_b32 v29, v63, v61, s61\n\t"
    "v_accvgpr_write_b32 a183, v29\n\t"
    "v_perm_b32 v112, v35, v33, s62\n\t"
    "v_perm_b32 v113, v39, v37, s62\n\t"
    "v_perm_b32 v114, v43, v41, s62\n\t"
    "v_perm_b32 v115, v47, v45, s62\n\t"
    "v_perm_b32 v116, v51, v49, s62\n\t"
    "v_perm_b32 v117, v55, v53, s62\n\t"
    "v_perm_b32 v118, v59, v57, s62\n\t"
    "v_perm_b32 v119, v63, v61, s62\n\t"
    "ds_read_b64 v[32:33], v28 offset:1024\n\t"
    "ds_read_b64 v[34:35], v28 offset:1280\n\t"
    "ds_read_b64 v[36:37], v28 offset:9216\n\t"
    "ds_read_b64 v[38:39], v28 offset:9472\n\t"
    "ds_read_b64 v[40:41], v28 offset:3072\n\t"
    "ds_read_b64 v[42:43], v28 offset:3328\n\t"
    "ds_read_b64 v[44:45], v28 offset:11264\n\t"
    "ds_read_b64 v[46:47], v28 offset:11520\n\t"
    "ds_read_b64 v[48:49], v28 offset:1536\n\t"
    "ds_read_b64 v[50:51], v28 offset:1792\n\t"
    "ds_read_b64 v[52:53], v28 offset:9728\n\t"
    "ds_read_b64 v[54:55], v28 offset:9984\n\t"
    "ds_read_b64 v[56:57], v28 offset:3584\n\t"
    "ds_read_b64 v[58:59], v28 offset:3840\n\t"
    "ds_read_b64 v[60:61], v28 offset:11776\n\t"
    "ds_read_b64 v[62:63], v28 offset:12032\n\t"
    "s_waitcnt lgkmcnt(14)\n\t"
    "v_perm_b32 v29, v34, v32, s61\n\t"
    "v_accvgpr_write_b32 a56, v29\n\t"
    "s_waitcnt lgkmcnt(12)\n\t"
    "v_perm_b32 v29, v38, v36, s61\n\t"
    "v_accvgpr_write_b32 a57, v29\n\t"
    "s_waitcnt lgkmcnt(10)\n\t"
    "v_perm_b32 v29, v42, v40, s61\n\t"
    "v_accvgpr_write_b32 a58, v29\n\t"
    "s_waitcnt lgkmcnt(8)\n\t"
    "v_perm_b32 v29, v46, v44, s61\n\t"
    "v_accvgpr_write_b32 a59, v29\n\t"
    "s_waitcnt lgkmcnt(6)\n\t"
    "v_perm_b32 v29, v50, v48, s61\n\t"
    "v_accvgpr_write_b32 a60, v29\n\t"
    "s_waitcnt lgkmcnt(4)\n\t"
    "v_perm_b32 v29, v54, v52, s61\n\t"
    "v_accvgpr_write_b32 a61, v29\n\t"
    "s_waitcnt lgkmcnt(2)\n\t"
    "v_perm_b32 v29, v58, v56, s61\n\t"
    "v_accvgpr_write_b32 a62, v29\n\t"
    "s_waitcnt lgkmcnt(0)\n\t"
    "v_perm_b32 v29, v62, v60, s61\n\t"
    "v_accvgpr_write_b32 a63, v29\n\t"
    "v_perm_b32 v29, v34, v32, s62\n\t"
    "v_accvgpr_write_b32 a120, v29\n\t"
    "v_perm_b32 v29, v38, v36, s62\n\t"
    "v_accvgpr_write_b32 a121, v29\n\t"
    "v_perm_b32 v29, v42, v40, s62\n\t"
    "v_accvgpr_write_b32 a122, v29\n\t"
    "v_perm_b32 v29, v46, v44, s62\n\t"
    "v_accvgpr_write_b32 a123, v29\n\t"
    "v_perm_b32 v29, v50, v48, s62\n\t"
    "v_accvgpr_write_b32 a124, v29\n\t"
    "v_perm_b32 v29, v54, v52, s62\n\t"
    "v_accvgpr_write_b32 a125, v29\n\t"
    "v_perm_b32 v29, v58, v56, s62\n\t"
    "v_accvgpr_write_b32 a126, v29\n\t"
    "v_perm_b32 v29, v62, v60, s62\n\t"
    "v_accvgpr_write_b32 a127, v29\n\t"
    "v_perm_b32 v29, v35, v33, s61\n\t"
    "v_accvgpr_write_b32 a184, v29\n\t"
    "v_perm_b32 v29, v39, v37, s61\n\t"
    "v_accvgpr_write_b32 a185, v29\n\t"
    "v_perm_b32 v29, v43, v41, s61\n\t"
    "v_accvgpr_write_b32 a186, v29\n\t"
    "v_perm_b32 v29, v47, v45, s61\n\t"
    "v_accvgpr_write_b32 a187, v29\n\t"
    "v_perm_b32 v29, v51, v49, s61\n\t"
    "v_accvgpr_write_b32 a188, v29\n\t"
    "v_perm_b32 v29, v55, v53, s61\n\t"
    "v_accvgpr_write_b32 a189, v29\n\t"
    "v_perm_b32 v29, v59, v57, s61\n\t"
    "v_accvgpr_write_b32 a190, v29\n\t"
    "v_perm_b32 v29, v63, v61, s61\n\t"
    "v_accvgpr_write_b32 a191, v29\n\t"
    "v_perm_b32 v120, v35, v33, s62\n\t"
    "v_perm_b32 v121, v39, v37, s62\n\t"
    "v_perm_b32 v122, v43, v41, s62\n\t"
    "v_perm_b32 v123, v47, v45, s62\n\t"
    "v_perm_b32 v124, v51, v49, s62\n\t"
    "v_perm_b32 v125, v55, v53, s62\n\t"
    "v_perm_b32 v126, v59, v57, s62\n\t"
    "v_perm_b32 v127, v63, v61, s62\n\t"
    "s_waitcnt lgkmcnt(0)\n\t"
    "s_barrier\n\t"
    "s_lshl_b32 s55, s51, 14\n\t"
    "s_add_u32 s55, s55, 16384\n\t"
    "s_lshl_b32 s56, s52, 17\n\t"
    "s_mov_b32 s40, %[wo_lo]\n\t"
    "s_mov_b32 s41, %[wo_hi]\n\t"
    "s_add_u32 s40, s40, s56\n\t"
    "s_addc_u32 s41, s41, 0\n\t"
    "s_sub_u32 s42, 4194304, s56\n\t"
    "s_and_b32 s41, s41, 0xffff\n\t"
    "s_lshl_b32 s56, s51, 14\n\t"
    "s_add_u32 s56, s56, 0\n\t"
    "s_mov_b32 m0, s55\n\t"
    "s_nop 0\n\t"
    "buffer_load_dwordx4 v0, s[40:43], s56 offen lds\n\t"
    "s_add_u32 s55, s55, 1024\n\t"
    "s_add_u32 s56, s56, 1024\n\t"
    "s_mov_b32 m0, s55\n\t"
    "s_nop 0\n\t"
    "buffer_load_dwordx4 v0, s[40:43], s56 offen lds\n\t"
    "s_add_u32 s55, s55, 1024\n\t"
    "s_add_u32 s56, s56, 1024\n\t"
    "s_mov_b32 m0, s55\n\t"
    "s_nop 0\n\t"
    "buffer_load_dwordx4 v0, s[40:43], s56 offen lds\n\t"
    "s_add_u32 s55, s55, 1024\n\t"
    "s_add_u32 s56, s56, 1024\n\t"
    "s_mov_b32 m0, s55\n\t"
    "s_nop 0\n\t"
    "buffer_load_dwordx4 v0, s[40:43], s56 offen lds\n\t"
    "s_add_u32 s55, s55, 1024\n\t"
    "s_add_u32 s56, s56, 1024\n\t"
    "s_mov_b32 m0, s55\n\t"
    "s_nop 0\n\t"
    "buffer_load_dwordx4 v0, s[40:43], s56 offen lds\n\t"
    "s_add_u32 s55, s55, 1024\n\t"
    "s_add_u32 s56, s56, 1024\n\t"
    "s_mov_b32 m0, s55\n\t"
    "s_nop 0\n\t"
    "buffer_load_dwordx4 v0, s[40:43], s56 offen lds\n\t"
    "s_add_u32 s55, s55, 1024\n\t"
    "s_add_u32 s56, s56, 1024\n\t"
    "s_mov_b32 m0, s55\n\t"
    "s_nop 0\n\t"
    "buffer_load_dwordx4 v0, s[40:43], s56 offen lds\n\t"
    "s_add_u32 s55, s55, 1024\n\t"
    "s_add_u32 s56, s56, 1024\n\t"
    "s_mov_b32 m0, s55\n\t"
    "s_nop 0\n\t"
    "buffer_load_dwordx4 v0, s[40:43], s56 offen lds\n\t"
    "s_add_u32 s55, s55, 1024\n\t"
    "s_add_u32 s56, s56, 1024\n\t"
    "s_mov_b32 m0, s55\n\t"
    "s_nop 0\n\t"
    "buffer_load_dwordx4 v0, s[40:43], s56 offen lds\n\t"
    "s_add_u32 s55, s55, 1024\n\t"
    "s_add_u32 s56, s56, 1024\n\t"
    "s_mov_b32 m0, s55\n\t"
    "s_nop 0\n\t"
    "buffer_load_dwordx4 v0, s[40:43], s56 offen lds\n\t"
    "s_add_u32 s55, s55, 1024\n\t"
    "s_add_u32 s56, s56, 1024\n\t"
    "s_mov_b32 m0, s55\n\t"
    "s_nop 0\n\t"
    "buffer_load_dwordx4 v0, s[40:43], s56 offen lds\n\t"
    "s_add_u32 s55, s55, 1024\n\t"
    "s_add_u32 s56, s56, 1024\n\t"
    "s_mov_b32 m0, s55\n\t"
    "s_nop 0\n\t"
    "buffer_load_dwordx4 v0, s[40:43], s56 offen lds\n\t"
    "s_add_u32 s55, s55, 1024\n\t"
    "s_add_u32 s56, s56, 1024\n\t"
    "s_mov_b32 m0, s55\n\t"
    "s_nop 0\n\t"
    "buffer_load_dwordx4 v0, s[40:43], s56 offen lds\n\t"
    "s_add_u32 s55, s55, 1024\n\t"
    "s_add_u32 s56, s56, 1024\n\t"
    "s_mov_b32 m0, s55\n\t"
    "s_nop 0\n\t"
    "buffer_load_dwordx4 v0, s[40:43], s56 offen lds\n\t"
    "s_add_u32 s55, s55, 1024\n\t"
    "s_add_u32 s56, s56, 1024\n\t"
    "s_mov_b32 m0, s55\n\t"
    "s_nop 0\n\t"
    "buffer_load_dwordx4 v0, s[40:43], s56 offen lds\n\t"
    "s_add_u32 s55, s55, 1024\n\t"
    "s_add_u32 s56, s56, 1024\n\t"
    "s_mov_b32 m0, s55\n\t"
    "s_nop 0\n\t"
    "buffer_load_dwordx4 v0, s[40:43], s56 offen lds\n\t"
    "s_waitcnt vmcnt(16)\n\t"
    "s_barrier\n\t"
    "s_lshl_b32 s55, s51, 6\n\t"
    "s_add_u32 s55, s55, 81920\n\t"
    "v_lshlrev_b32 v28, 8, v27\n\t"
    "v_lshl_add_u32 v28, v17, 2, v28\n\t"
    "v_add_u32 v28, s55, v28\n\t"
    "ds_read_b32 v32, v28 offset:0\n\t"
    "ds_read_b32 v33, v28 offset:256\n\t"
    "ds_read_b32 v34, v28 offset:8192\n\t"
    "ds_read_b32 v35, v28 offset:8448\n\t"
    "ds_read_b32 v36, v28 offset:2048\n\t"
    "ds_read_b32 v37, v28 offset:2304\n\t"
    "ds_read_b32 v38, v28 offset:10240\n\t"
    "ds_read_b32 v39, v28 offset:10496\n\t"
    "ds_read_b32 v40, v28 offset:512\n\t"
    "ds_read_b32 v41, v28 offset:768\n\t"
    "ds_read_b32 v42, v28 offset:8704\n\t"
    "ds_read_b32 v43, v28 offset:8960\n\t"
    "ds_read_b32 v44, v28 offset:2560\n\t"
    "ds_read_b32 v45, v28 offset:2816\n\t"
    "ds_read_b32 v46, v28 offset:10752\n\t"
    "ds_read_b32 v47, v28 offset:11008\n\t"
    "s_waitcnt lgkmcnt(14)\n\t"
    "v_perm_b32 v192, v33, v32, s61\n\t"
    "s_waitcnt lgkmcnt(12)\n\t"
    "v_perm_b32 v193, v35, v34, s61\n\t"
    "s_waitcnt lgkmcnt(10)\n\t"
    "v_perm_b32 v194, v37, v36, s61\n\t"
    "s_waitcnt lgkmcnt(8)\n\t"
    "v_perm_b32 v195, v39, v38, s61\n\t"
    "s_waitcnt lgkmcnt(6)\n\t"
    "v_perm_b32 v196, v41, v40, s61\n\t"
    "s_waitcnt lgkmcnt(4)\n\t"
    "v_perm_b32 v197, v43, v42, s61\n\t"
    "s_waitcnt lgkmcnt(2)\n\t"
    "v_perm_b32 v198, v45, v44, s61\n\t"
    "s_waitcnt lgkmcnt(0)\n\t"
    "v_perm_b32 v199, v47, v46, s61\n\t"
    "v_perm_b32 v200, v33, v32, s62\n\t"
    "v_perm_b32 v201, v35, v34, s62\n\t"
    "v_perm_b32 v202, v37, v36, s62\n\t"
    "v_perm_b32 v203, v39, v38, s62\n\t"
    "v_perm_b32 v204, v41, v40, s62\n\t"
    "v_perm_b32 v205, v43, v42, s62\n\t"
    "v_perm_b32 v206, v45, v44, s62\n\t"
    "v_perm_b32 v207, v47, v46, s62\n\t"
    "ds_read_b32 v32, v28 offset:1024\n\t"
    "ds_read_b32 v33, v28 offset:1280\n\t"
    "ds_read_b32 v34, v28 offset:9216\n\t"
    "ds_read_b32 v35, v28 offset:9472\n\t"
    "ds_read_b32 v36, v28 offset:3072\n\t"
    "ds_read_b32 v37, v28 offset:3328\n\t"
    "ds_read_b32 v38, v28 offset:11264\n\t"
    "ds_read_b32 v39, v28 offset:11520\n\t"
    "ds_read_b32 v40, v28 offset:1536\n\t"
    "ds_read_b32 v41, v28 offset:1792\n\t"
    "ds_read_b32 v42, v28 offset:9728\n\t"
    "ds_read_b32 v43, v28 offset:9984\n\t"
    "ds_read_b32 v44, v28 offset:3584\n\t"
    "ds_read_b32 v45, v28 offset:3840\n\t"
    "ds_read_b32 v46, v28 offset:11776\n\t"
    "ds_read_b32 v47, v28 offset:12032\n\t"
    "s_waitcnt lgkmcnt(14)\n\t"
    "v_perm_b32 v208, v33, v32, s61\n\t"
    "s_waitcnt lgkmcnt(12)\n\t"
    "v_perm_b32 v209, v35, v34, s61\n\t"
    "s_waitcnt lgkmcnt(10)\n\t"
    "v_perm_b32 v210, v37, v36, s61\n\t"
    "s_waitcnt lgkmcnt(8)\n\t"
    "v_perm_b32 v211, v39, v38, s61\n\t"
    "s_waitcnt lgkmcnt(6)\n\t"
    "v_perm_b32 v212, v41, v40, s61\n\t"
    "s_waitcnt lgkmcnt(4)\n\t"
    "v_perm_b32 v213, v43, v42, s61\n\t"
    "s_waitcnt lgkmcnt(2)\n\t"
    "v_perm_b32 v214, v45, v44, s61\n\t"
    "s_waitcnt lgkmcnt(0)\n\t"
    "v_perm_b32 v215, v47, v46, s61\n\t"
    "v_perm_b32 v216, v33, v32, s62\n\t"
    "v_perm_b32 v217, v35, v34, s62\n\t"
    "v_perm_b32 v218, v37, v36, s62\n\t"
    "v_perm_b32 v219, v39, v38, s62\n\t"
    "v_perm_b32 v220, v41, v40, s62\n\t"
    "v_perm_b32 v221, v43, v42, s62\n\t"
    "v_perm_b32 v222, v45, v44, s62\n\t"
    "v_perm_b32 v223, v47, v46, s62\n\t"
    "ds_read_b32 v32, v28 offset:32768\n\t"
    "ds_read_b32 v33, v28 offset:33024\n\t"
    "ds_read_b32 v34, v28 offset:40960\n\t"
    "ds_read_b32 v35, v28 offset:41216\n\t"
    "ds_read_b32 v36, v28 offset:34816\n\t"
    "ds_read_b32 v37, v28 offset:35072\n\t"
    "ds_read_b32 v38, v28 offset:43008\n\t"
    "ds_read_b32 v39, v28 offset:43264\n\t"
    "ds_read_b32 v40, v28 offset:33280\n\t"
    "ds_read_b32 v41, v28 offset:33536\n\t"
    "ds_read_b32 v42, v28 offset:41472\n\t"
    "ds_read_b32 v43, v28 offset:41728\n\t"
    "ds_read_b32 v44, v28 offset:35328\n\t"
    "ds_read_b32 v45, v28 offset:35584\n\t"
    "ds_read_b32 v46, v28 offset:43520\n\t"
    "ds_read_b32 v47, v28 offset:43776\n\t"
    "s_waitcnt lgkmcnt(14)\n\t"
    "v_perm_b32 v224, v33, v32, s61\n\t"
    "s_waitcnt lgkmcnt(12)\n\t"
    "v_perm_b32 v225, v35, v34, s61\n\t"
    "s_waitcnt lgkmcnt(10)\n\t"
    "v_perm_b32 v226, v37, v36, s61\n\t"
    "s_waitcnt lgkmcnt(8)\n\t"
    "v_perm_b32 v227, v39, v38, s61\n\t"
    "s_waitcnt lgkmcnt(6)\n\t"
    "v_perm_b32 v228, v41, v40, s61\n\t"
    "s_waitcnt lgkmcnt(4)\n\t"
    "v_perm_b32 v229, v43, v42, s61\n\t"
    "s_waitcnt lgkmcnt(2)\n\t"
    "v_perm_b32 v230, v45, v44, s61\n\t"
    "s_waitcnt lgkmcnt(0)\n\t"
    "v_perm_b32 v231, v47, v46, s61\n\t"
    "v_perm_b32 v232, v33, v32, s62\n\t"
    "v_perm_b32 v233, v35, v34, s62\n\t"
    "v_perm_b32 v234, v37, v36, s62\n\t"
    "v_perm_b32 v235, v39, v38, s62\n\t"
    "v_perm_b32 v236, v41, v40, s62\n\t"
    "v_perm_b32 v237, v43, v42, s62\n\t"
    "v_perm_b32 v238, v45, v44, s62\n\t"
    "v_perm_b32 v239, v47, v46, s62\n\t"
    "ds_read_b32 v32, v28 offset:33792\n\t"
    "ds_read_b32 v33, v28 offset:34048\n\t"
    "ds_read_b32 v34, v28 offset:41984\n\t"
    "ds_read_b32 v35, v28 offset:42240\n\t"
    "ds_read_b32 v36, v28 offset:35840\n\t"
    "ds_read_b32 v37, v28 offset:36096\n\t"
    "ds_read_b32 v38, v28 offset:44032\n\t"
    "ds_read_b32 v39, v28 offset:44288\n\t"
    "ds_read_b32 v40, v28 offset:34304\n\t"
    "ds_read_b32 v41, v28 offset:34560\n\t"
    "ds_read_b32 v42, v28 offset:42496\n\t"
    "ds_read_b32 v43, v28 offset:42752\n\t"
    "ds_read_b32 v44, v28 offset:36352\n\t"
    "ds_read_b32 v45, v28 offset:36608\n\t"
    "ds_read_b32 v46, v28 offset:44544\n\t"
    "ds_read_b32 v47, v28 offset:44800\n\t"
    "s_waitcnt lgkmcnt(14)\n\t"
    "v_perm_b32 v240, v33, v32, s61\n\t"
    "s_waitcnt lgkmcnt(12)\n\t"
    "v_perm_b32 v241, v35, v34, s61\n\t"
    "s_waitcnt lgkmcnt(10)\n\t"
    "v_perm_b32 v242, v37, v36, s61\n\t"
    "s_waitcnt lgkmcnt(8)\n\t"
    "v_perm_b32 v243, v39, v38, s61\n\t"
    "s_waitcnt lgkmcnt(6)\n\t"
    "v_perm_b32 v244, v41, v40, s61\n\t"
    "s_waitcnt lgkmcnt(4)\n\t"
    "v_perm_b32 v245, v43, v42, s61\n\t"
    "s_waitcnt lgkmcnt(2)\n\t"
    "v_perm_b32 v246, v45, v44, s61\n\t"
    "s_waitcnt lgkmcnt(0)\n\t"
    "v_perm_b32 v247, v47, v46, s61\n\t"
    "v_perm_b32 v248, v33, v32, s62\n\t"
    "v_perm_b32 v249, v35, v34, s62\n\t"
    "v_perm_b32 v250, v37, v36, s62\n\t"
    "v_perm_b32 v251, v39, v38, s62\n\t"
    "v_perm_b32 v252, v41, v40, s62\n\t"
    "v_perm_b32 v253, v43, v42, s62\n\t"
    "v_perm_b32 v254, v45, v44, s62\n\t"
    "v_perm_b32 v255, v47, v46, s62\n\t"
    "s_waitcnt lgkmcnt(0)\n\t"
    "s_barrier\n\t"
    "s_lshl_b32 s55, s51, 14\n\t"
    "s_add_u32 s55, s55, 81920\n\t"
    "s_lshl_b32 s56, s52, 17\n\t"
    "s_mov_b32 s40, %[wo_lo]\n\t"
    "s_mov_b32 s41, %[wo_hi]\n\t"
    "s_add_u32 s40, s40, s56\n\t"
    "s_addc_u32 s41, s41, 0\n\t"
    "s_sub_u32 s42, 4194304, s56\n\t"
    "s_and_b32 s41, s41, 0xffff\n\t"
    "s_lshl_b32 s56, s51, 14\n\t"
    "s_add_u32 s56, s56, 65536\n\t"
    "s_mov_b32 m0, s55\n\t"
    "s_nop 0\n\t"
    "buffer_load_dwordx4 v0, s[40:43], s56 offen lds\n\t"
    "s_add_u32 s55, s55, 1024\n\t"
    "s_add_u32 s56, s56, 1024\n\t"
    "s_mov_b32 m0, s55\n\t"
    "s_nop 0\n\t"
    "buffer_load_dwordx4 v0, s[40:43], s56 offen lds\n\t"
    "s_add_u32 s55, s55, 1024\n\t"
    "s_add_u32 s56, s56, 1024\n\t"
    "s_mov_b32 m0, s55\n\t"
    "s_nop 0\n\t"
    "buffer_load_dwordx4 v0, s[40:43], s56 offen lds\n\t"
    "s_add_u32 s55, s55, 1024\n\t"
    "s_add_u32 s56, s56, 1024\n\t"
    "s_mov_b32 m0, s55\n\t"
    "s_nop 0\n\t"
    "buffer_load_dwordx4 v0, s[40:43], s56 offen lds\n\t"
    "s_add_u32 s55, s55, 1024\n\t"
    "s_add_u32 s56, s56, 1024\n\t"
    "s_mov_b32 m0, s55\n\t"
    "s_nop 0\n\t"
    "buffer_load_dwordx4 v0, s[40:43], s56 offen lds\n\t"
    "s_add_u32 s55, s55, 1024\n\t"
    "s_add_u32 s56, s56, 1024\n\t"
    "s_mov_b32 m0, s55\n\t"
    "s_nop 0\n\t"
    "buffer_load_dwordx4 v0, s[40:43], s56 offen lds\n\t"
    "s_add_u32 s55, s55, 1024\n\t"
    "s_add_u32 s56, s56, 1024\n\t"
    "s_mov_b32 m0, s55\n\t"
    "s_nop 0\n\t"
    "buffer_load_dwordx4 v0, s[40:43], s56 offen lds\n\t"
    "s_add_u32 s55, s55, 1024\n\t"
    "s_add_u32 s56, s56, 1024\n\t"
    "s_mov_b32 m0, s55\n\t"
    "s_nop 0\n\t"
    "buffer_load_dwordx4 v0, s[40:43], s56 offen lds\n\t"
    "s_add_u32 s55, s55, 1024\n\t"
    "s_add_u32 s56, s56, 1024\n\t"
    "s_mov_b32 m0, s55\n\t"
    "s_nop 0\n\t"
    "buffer_load_dwordx4 v0, s[40:43], s56 offen lds\n\t"
    "s_add_u32 s55, s55, 1024\n\t"
    "s_add_u32 s56, s56, 1024\n\t"
    "s_mov_b32 m0, s55\n\t"
    "s_nop 0\n\t"
    "buffer_load_dwordx4 v0, s[40:43], s56 offen lds\n\t"
    "s_add_u32 s55, s55, 1024\n\t"
    "s_add_u32 s56, s56, 1024\n\t"
    "s_mov_b32 m0, s55\n\t"
    "s_nop 0\n\t"
    "buffer_load_dwordx4 v0, s[40:43], s56 offen lds\n\t"
    "s_add_u32 s55, s55, 1024\n\t"
    "s_add_u32 s56, s56, 1024\n\t"
    "s_mov_b32 m0, s55\n\t"
    "s_nop 0\n\t"
    "buffer_load_dwordx4 v0, s[40:43], s56 offen lds\n\t"
    "s_add_u32 s55, s55, 1024\n\t"
    "s_add_u32 s56, s56, 1024\n\t"
    "s_mov_b32 m0, s55\n\t"
    "s_nop 0\n\t"
    "buffer_load_dwordx4 v0, s[40:43], s56 offen lds\n\t"
    "s_add_u32 s55, s55, 1024\n\t"
    "s_add_u32 s56, s56, 1024\n\t"
    "s_mov_b32 m0, s55\n\t"
    "s_nop 0\n\t"
    "buffer_load_dwordx4 v0, s[40:43], s56 offen lds\n\t"
    "s_add_u32 s55, s55, 1024\n\t"
    "s_add_u32 s56, s56, 1024\n\t"
    "s_mov_b32 m0, s55\n\t"
    "s_nop 0\n\t"
    "buffer_load_dwordx4 v0, s[40:43], s56 offen lds\n\t"
    "s_add_u32 s55, s55, 1024\n\t"
    "s_add_u32 s56, s56, 1024\n\t"
    "s_mov_b32 m0, s55\n\t"
    "s_nop 0\n\t"
    "buffer_load_dwordx4 v0, s[40:43], s56 offen lds\n\t"
    "v_lshlrev_b32 v29, 7, v16\n\t"
    "v_add_u32 v29, 2142208, v29\n\t"
    "buffer_load_dwordx2 v[30:31], v29, s[36:39], 0 offen sc1\n\t"
    "s_lshr_b32 s56, s52, 1\n\t"
    "s_lshl_b32 s56, s56, 7\n\t"
    "s_add_u32 s56, s56, 2121728\n\t"
    "v_mov_b32 v29, 2117632\n\t"
    "v_mov_b32 v28, s56\n\t"
    "v_cmp_eq_u32 vcc, 1, v16\n\t"
    "s_nop 1\n\t"
    "v_cndmask_b32 v29, v29, v28, vcc\n\t"
    "v_add_u32 v28, 2048, v28\n\t"
    "v_cmp_eq_u32 vcc, 2, v16\n\t"
    "s_nop 1\n\t"
    "v_cndmask_b32 v29, v29, v28, vcc\n\t"
    "buffer_load_dwordx2 v[24:25], v29, s[36:39], 0 offen sc1\n\t"
    "buffer_load_dword v26, v15, s[36:39], 0 offen sc1\n\t"
    "s_waitcnt vmcnt(19)\n\t"
    "s_barrier\n\t"
    "s_lshl_b32 s55, s51, 8\n\t"
    "s_add_u32 s55, s55, 16384\n\t"
    "v_lshrrev_b32 v29, 1, v18\n\t"
    "v_lshlrev_b32 v28, 14, v29\n\t"
    "v_lshl_add_u32 v28, v19, 12, v28\n\t"
    "v_lshl_add_u32 v28, v17, 4, v28\n\t"
    "v_add_u32 v28, s55, v28\n\t"
    "ds_read_b128 v[32:35], v28 offset:0\n\t"
    "ds_read_b128 v[36:39], v28 offset:1024\n\t"
    "ds_read_b128 v[40:43], v28 offset:8192\n\t"
    "ds_read_b128 v[44:47], v28 offset:9216\n\t"
    "ds_read_b128 v[48:51], v28 offset:2048\n\t"
    "ds_read_b128 v[52:55], v28 offset:3072\n\t"
    "ds_read_b128 v[56:59], v28 offset:10240\n\t"
    "ds_read_b128 v[60:63], v28 offset:11264\n\t"
    "s_waitcnt lgkmcnt(6)\n\t"
    "v_perm_b32 v128, v36, v32, s61\n\t"
    "s_waitcnt lgkmcnt(4)\n\t"
    "v_perm_b32 v129, v44, v40, s61\n\t"
    "s_waitcnt lgkmcnt(2)\n\t"
    "v_perm_b32 v130, v52, v48, s61\n\t"
    "s_waitcnt lgkmcnt(0)\n\t"
    "v_perm_b32 v131, v60, v56, s61\n\t"
    "v_perm_b32 v136, v36, v32, s62\n\t"
    "v_perm_b32 v137, v44, v40, s62\n\t"
    "v_perm_b32 v138, v52, v48, s62\n\t"
    "v_perm_b32 v139, v60, v56, s62\n\t"
    "v_perm_b32 v144, v37, v33, s61\n\t"
    "v_perm_b32 v145, v45, v41, s61\n\t"
    "v_perm_b32 v146, v53, v49, s61\n\t"
    "v_perm_b32 v147, v61, v57, s61\n\t"
    "v_perm_b32 v152, v37, v33, s62\n\t"
    "v_perm_b32 v153, v45, v41, s62\n\t"
    "v_perm_b32 v154, v53, v49, s62\n\t"
    "v_perm_b32 v155, v61, v57, s62\n\t"
    "v_perm_b32 v160, v38, v34, s61\n\t"
    "v_perm_b32 v161, v46, v42, s61\n\t"
    "v_perm_b32 v162, v54, v50, s61\n\t"
    "v_perm_b32 v163, v62, v58, s61\n\t"
    "v_perm_b32 v168, v38, v34, s62\n\t"
    "v_perm_b32 v169, v46, v42, s62\n\t"
    "v_perm_b32 v170, v54, v50, s62\n\t"
    "v_perm_b32 v171, v62, v58, s62\n\t"
    "v_perm_b32 v176, v39, v35, s61\n\t"
    "v_perm_b32 v177, v47, v43, s61\n\t"
    "v_perm_b32 v178, v55, v51, s61\n\t"
    "v_perm_b32 v179, v63, v59, s61\n\t"
    "v_perm_b32 v184, v39, v35, s62\n\t"
    "v_perm_b32 v185, v47, v43, s62\n\t"
    "v_perm_b32 v186, v55, v51, s62\n\t"
    "v_perm_b32 v187, v63, v59, s62\n\t"
    "ds_read_b128 v[32:35], v28 offset:32768\n\t"
    "ds_read_b128 v[36:39], v28 offset:33792\n\t"
    "ds_read_b128 v[40:43], v28 offset:40960\n\t"
    "ds_read_b128 v[44:47], v28 offset:41984\n\t"
    "ds_read_b128 v[48:51], v28 offset:34816\n\t"
    "ds_read_b128 v[52:55], v28 offset:35840\n\t"
    "ds_read_b128 v[56:59], v28 offset:43008\n\t"
    "ds_read_b128 v[60:63], v28 offset:44032\n\t"
    "s_waitcnt lgkmcnt(6)\n\t"
    "v_perm_b32 v132, v36, v32, s61\n\t"
    "s_waitcnt lgkmcnt(4)\n\t"
    "v_perm_b32 v133, v44, v40, s61\n\t"
    "s_waitcnt lgkmcnt(2)\n\t"
    "v_perm_b32 v134, v52, v48, s61\n\t"
    "s_waitcnt lgkmcnt(0)\n\t"
    "v_perm_b32 v135, v60, v56, s61\n\t"
    "v_perm_b32 v140, v36, v32, s62\n\t"
    "v_perm_b32 v141, v44, v40, s62\n\t"
    "v_perm_b32 v142, v52, v48, s62\n\t"
    "v_perm_b32 v143, v60, v56, s62\n\t"
    "v_perm_b32 v148, v37, v33, s61\n\t"
    "v_perm_b32 v149, v45, v41, s61\n\t"
    "v_perm_b32 v150, v53, v49, s61\n\t"
    "v_perm_b32 v151, v61, v57, s61\n\t"
    "v_perm_b32 v156, v37, v33, s62\n\t"
    "v_perm_b32 v157, v45, v41, s62\n\t"
    "v_perm_b32 v158, v53, v49, s62\n\t"
    "v_perm_b32 v159, v61, v57, s62\n\t"
    "v_perm_b32 v164, v38, v34, s61\n\t"
    "v_perm_b32 v165, v46, v42, s61\n\t"
    "v_perm_b32 v166, v54, v50, s61\n\t"
    "v_perm_b32 v167, v62, v58, s61\n\t"
    "v_perm_b32 v172, v38, v34, s62\n\t"
    "v_perm_b32 v173, v46, v42, s62\n\t"
    "v_perm_b32 v174, v54, v50, s62\n\t"
    "v_perm_b32 v175, v62, v58, s62\n\t"
    "v_perm_b32 v180, v39, v35, s61\n\t"
    "v_perm_b32 v181, v47, v43, s61\n\t"
    "v_perm_b32 v182, v55, v51, s61\n\t"
    "v_perm_b32 v183, v63, v59, s61\n\t"
    "v_perm_b32 v188, v39, v35, s62\n\t"
    "v_perm_b32 v189, v47, v43, s62\n\t"
    "v_perm_b32 v190, v55, v51, s62\n\t"
    "v_perm_b32 v191, v63, v59, s62\n\t"
    "s_waitcnt lgkmcnt(0)\n\t"
    "s_barrier\n\t"
    "ds_write_b128 v2, v[64:67] offset:0\n\t"
    "ds_write_b128 v2, v[68:71] offset:1024\n\t"
    "ds_write_b128 v2, v[72:75] offset:8192\n\t"
    "ds_write_b128 v2, v[76:79] offset:9216\n\t"
    "ds_write_b128 v2, v[80:83] offset:16384\n\t"
    "ds_write_b128 v2, v[84:87] offset:17408\n\t"
    "ds_write_b128 v2, v[88:91] offset:24576\n\t"
    "ds_write_b128 v2, v[92:95] offset:25600\n\t"
    "ds_write_b128 v2, v[96:99] offset:32768\n\t"
    "ds_write_b128 v2, v[100:103] offset:33792\n\t"
    "ds_write_b128 v2, v[104:107] offset:40960\n\t"
    "ds_write_b128 v2, v[108:111] offset:41984\n\t"
    "ds_write_b128 v2, v[112:115] offset:49152\n\t"
    "ds_write_b128 v2, v[116:119] offset:50176\n\t"
    "ds_write_b128 v2, v[120:123] offset:57344\n\t"
    "ds_write_b128 v2, v[124:127] offset:58368\n\t"
    "s_waitcnt lgkmcnt(0)\n\t"
    "s_waitcnt vmcnt(3)\n\t"
    "s_barrier\n\t"
    "s_waitcnt vmcnt(0)\n\t"
    "v_cmp_ne_u32 vcc, s67, v31\n\t"
    "s_cbranch_vccnz HSLOW%=\n\t"
    "v_cmp_ne_u32 vcc, s67, v25\n\t"
    "s_cbranch_vccnz HSLOW%=\n\t"
    "v_readlane_b32 s55, v26, 0\n\t"
    "v_readlane_b32 s57, v26, 1\n\t"
    "s_min_i32 s64, s55, s57\n\t"
    "s_cmp_lt_i32 s64, 0\n\t"
    "s_cbranch_scc1 HSLOW%=\n\t"
    "v_readfirstlane_b32 s53, v24\n\t"
    "s_mov_b32 s44, 0\n\t"
    "s_lshl_b32 s53, s53, 13\n\t"
    "s_add_u32 s45, s53, 16384\n\t"
    "s_add_u32 s46, s53, 1048576\n\t"
    "s_lshl_b32 s55, s52, 13\n\t"
    "s_add_u32 s48, s55, 25165824\n\t"
    "s_add_u32 s49, s55, 41943040\n\t"
    "s_lshl_b32 s55, s52, 12\n\t"
    "s_add_u32 s50, s55, 67108864\n\t"
    "s_lshl_b32 s57, s51, 11\n\t"
    "s_add_u32 s65, s57, 147456\n\t"
    "s_mov_b32 m0, s65\n\t"
    "s_nop 0\n\t"
    "buffer_load_dwordx4 v1, s[36:39], s48 offen sc1 lds\n\t"
    "buffer_load_dwordx4 v1, s[36:39], s48 offen offset:1024 sc1 lds\n\t"
    "s_add_u32 s65, s57, 155648\n\t"
    "s_mov_b32 m0, s65\n\t"
    "s_nop 0\n\t"
    "buffer_load_dwordx4 v1, s[36:39], s49 offen sc1 lds\n\t"
    "buffer_load_dwordx4 v1, s[36:39], s49 offen offset:1024 sc1 lds\n\t"
    "s_add_u32 s48, s48, 262144\n\t"
    "s_add_u32 s49, s49, 262144\n\t"
    "s_mov_b32 s71, 1\n\t"
    "HSLOW%=:\n\t"
    "s_lshl_b32 s55, s51, 8\n\t"
    "s_add_u32 s55, s55, 81920\n\t"
    "v_lshrrev_b32 v29, 1, v18\n\t"
    "v_lshlrev_b32 v28, 14, v29\n\t"
    "v_lshl_add_u32 v28, v19, 12, v28\n\t"
    "v_lshl_add_u32 v28, v17, 4, v28\n\t"
    "v_add_u32 v28, s55, v28\n\t"
    "ds_read_b128 v[32:35], v28 offset:0\n\t"
    "ds_read_b128 v[36:39], v28 offset:1024\n\t"
    "ds_read_b128 v[40:43], v28 offset:8192\n\t"
    "ds_read_b128 v[44:47], v28 offset:9216\n\t"
    "ds_read_b128 v[48:51], v28 offset:2048\n\t"
    "ds_read_b128 v[52:55], v28 offset:3072\n\t"
    "ds_read_b128 v[56:59], v28 offset:10240\n\t"
    "ds_read_b128 v[60:63], v28 offset:11264\n\t"
    "s_waitcnt lgkmcnt(6)\n\t"
    "v_perm_b32 v64, v36, v32, s61\n\t"
    "s_waitcnt lgkmcnt(4)\n\t"
    "v_perm_b32 v65, v44, v40, s61\n\t"
    "s_waitcnt lgkmcnt(2)\n\t"
    "v_perm_b32 v66, v52, v48, s61\n\t"
    "s_waitcnt lgkmcnt(0)\n\t"
    "v_perm_b32 v67, v60, v56, s61\n\t"
    "v_perm_b32 v72, v36, v32, s62\n\t"
    "v_perm_b32 v73, v44, v40, s62\n\t"
    "v_perm_b32 v74, v52, v48, s62\n\t"
    "v_perm_b32 v75, v60, v56, s62\n\t"
    "v_perm_b32 v80, v37, v33, s61\n\t"
    "v_perm_b32 v81, v45, v41, s61\n\t"
    "v_perm_b32 v82, v53, v49, s61\n\t"
    "v_perm_b32 v83, v61, v57, s61\n\t"
    "v_perm_b32 v88, v37, v33, s62\n\t"
    "v_perm_b32 v89, v45, v41, s62\n\t"
    "v_perm_b32 v90, v53, v49, s62\n\t"
    "v_perm_b32 v91, v61, v57, s62\n\t"
    "v_perm_b32 v96, v38, v34, s61\n\t"
    "v_perm_b32 v97, v46, v42, s61\n\t"
    "v_perm_b32 v98, v54, v50, s61\n\t"
    "v_perm_b32 v99, v62, v58, s61\n\t"
    "v_perm_b32 v104, v38, v34, s62\n\t"
    "v_perm_b32 v105, v46, v42, s62\n\t"
    "v_perm_b32 v106, v54, v50, s62\n\t"
    "v_perm_b32 v107, v62, v58, s62\n\t"
    "v_perm_b32 v112, v39, v35, s61\n\t"
    "v_perm_b32 v113, v47, v43, s61\n\t"
    "v_perm_b32 v114, v55, v51, s61\n\t"
    "v_perm_b32 v115, v63, v59, s61\n\t"
    "v_perm_b32 v120, v39, v35, s62\n\t"
    "v_perm_b32 v121, v47, v43, s62\n\t"
    "v_perm_b32 v122, v55, v51, s62\n\t"
    "v_perm_b32 v123, v63, v59, s62\n\t"
    "ds_read_b128 v[32:35], v28 offset:32768\n\t"
    "ds_read_b128 v[36:39], v28 offset:33792\n\t"
    "ds_read_b128 v[40:43], v28 offset:40960\n\t"
    "ds_read_b128 v[44:47], v28 offset:41984\n\t"
    "ds_read_b128 v[48:51], v28 offset:34816\n\t"
    "ds_read_b128 v[52:55], v28 offset:35840\n\t"
    "ds_read_b128 v[56:59], v28 offset:43008\n\t"
    "ds_read_b128 v[60:63], v28 offset:44032\n\t"
    "s_waitcnt lgkmcnt(6)\n\t"
    "v_perm_b32 v68, v36, v32, s61\n\t"
    "s_waitcnt lgkmcnt(4)\n\t"
    "v_perm_b32 v69, v44, v40, s61\n\t"
    "s_waitcnt lgkmcnt(2)\n\t"
    "v_perm_b32 v70, v52, v48, s61\n\t"
    "s_waitcnt lgkmcnt(0)\n\t"
    "v_perm_b32 v71, v60, v56, s61\n\t"
    "v_perm_b32 v76, v36, v32, s62\n\t"
    "v_perm_b32 v77, v44, v40, s62\n\t"
    "v_perm_b32 v78, v52, v48, s62\n\t"
    "v_perm_b32 v79, v60, v56, s62\n\t"
    "v_perm_b32 v84, v37, v33, s61\n\t"
    "v_perm_b32 v85, v45, v41, s61\n\t"
    "v_perm_b32 v86, v53, v49, s61\n\t"
    "v_perm_b32 v87, v61, v57, s61\n\t"
    "v_perm_b32 v92, v37, v33, s62\n\t"
    "v_perm_b32 v93, v45, v41, s62\n\t"
    "v_perm_b32 v94, v53, v49, s62\n\t"
    "v_perm_b32 v95, v61, v57, s62\n\t"
    "v_perm_b32 v100, v38, v34, s61\n\t"
    "v_perm_b32 v101, v46, v42, s61\n\t"
    "v_perm_b32 v102, v54, v50, s61\n\t"
    "v_perm_b32 v103, v62, v58, s61\n\t"
    "v_perm_b32 v108, v38, v34, s62\n\t"
    "v_perm_b32 v109, v46, v42, s62\n\t"
    "v_perm_b32 v110, v54, v50, s62\n\t"
    "v_perm_b32 v111, v62, v58, s62\n\t"
    "v_perm_b32 v116, v39, v35, s61\n\t"
    "v_perm_b32 v117, v47, v43, s61\n\t"
    "v_perm_b32 v118, v55, v51, s61\n\t"
    "v_perm_b32 v119, v63, v59, s61\n\t"
    "v_perm_b32 v124, v39, v35, s62\n\t"
    "v_perm_b32 v125, v47, v43, s62\n\t"
    "v_perm_b32 v126, v55, v51, s62\n\t"
    "v_perm_b32 v127, v63, v59, s62\n\t"
    "s_waitcnt lgkmcnt(0)\n\t"
    "s_barrier\n\t"
    "ds_write_b128 v3, v[64:67] offset:0\n\t"
    "ds_write_b128 v3, v[68:71] offset:1024\n\t"
    "ds_write_b128 v3, v[72:75] offset:8192\n\t"
    "ds_write_b128 v3, v[76:79] offset:9216\n\t"
    "ds_write_b128 v3, v[80:83] offset:16384\n\t"
    "ds_write_b128 v3, v[84:87] offset:17408\n\t"
    "ds_write_b128 v3, v[88:91] offset:24576\n\t"
    "ds_write_b128 v3, v[92:95] offset:25600\n\t"
    "ds_write_b128 v3, v[96:99] offset:32768\n\t"
    "ds_write_b128 v3, v[100:103] offset:33792\n\t"
    "ds_write_b128 v3, v[104:107] offset:40960\n\t"
    "ds_write_b128 v3, v[108:111] offset:41984\n\t"
    "ds_write_b128 v3, v[112:115] offset:49152\n\t"
    "ds_write_b128 v3, v[116:119] offset:50176\n\t"
    "ds_write_b128 v3, v[120:123] offset:57344\n\t"
    "ds_write_b128 v3, v[124:127] offset:58368\n\t"
    "s_waitcnt lgkmcnt(0)\n\t"
    "s_barrier\n\t"
    "s_waitcnt lgkmcnt(0)\n\t"
    "s_cmp_eq_u32 s71, 1\n\t"
    "s_cbranch_scc1 HFASTD%=\n\t"
    "v_mbcnt_lo_u32_b32 v28, -1, 0\n\t"
    "v_mbcnt_hi_u32_b32 v28, -1, v28\n\t"
    "v_lshlrev_b32 v29, 7, v28\n\t"
    "v_add_u32 v29, 2142208, v29\n\t"
    "s_mov_b32 s54, 1000000\n\t"
    "HIR%=:\n\t"
    "buffer_load_dwordx2 v[30:31], v29, s[36:39], 0 offen sc1\n\t"
    "s_waitcnt vmcnt(0)\n\t"
    "v_cmp_ne_u32 vcc, s67, v31\n\t"
    "s_nop 1\n\t"
    "s_cbranch_vccz HIRd%=\n\t"
    "s_sub_u32 s54, s54, 1\n\t"
    "s_cbranch_scc1 HFAIL%=\n\t"
    "s_sleep 2\n\t"
    "s_branch HIR%=\n\t"
    "HIRd%=:\n\t"
    "s_lshr_b32 s56, s52, 1\n\t"
    "s_lshl_b32 s56, s56, 7\n\t"
    "s_add_u32 s56, s56, 2121728\n\t"
    "v_mov_b32 v29, 2117632\n\t"
    "v_mov_b32 v30, s56\n\t"
    "v_cmp_eq_u32 vcc, 1, v28\n\t"
    "s_nop 1\n\t"
    "v_cndmask_b32 v29, v29, v30, vcc\n\t"
    "v_add_u32 v30, 2048, v30\n\t"
    "v_cmp_eq_u32 vcc, 2, v28\n\t"
    "s_nop 1\n\t"
    "v_cndmask_b32 v29, v29, v30, vcc\n\t"
    "s_mov_b32 s54, 1000000\n\t"
    "HSEL%=:\n\t"
    "buffer_load_dwordx2 v[30:31], v29, s[36:39], 0 offen sc1\n\t"
    "s_waitcnt vmcnt(0)\n\t"
    "v_cmp_ne_u32 vcc, s67, v31\n\t"
    "s_cbranch_vccz HSELd%=\n\t"
    "s_sub_u32 s54, s54, 1\n\t"
    "s_cbranch_scc1 HFAIL%=\n\t"
    "s_sleep 2\n\t"
    "s_branch HSEL%=\n\t"
    "HSELd%=:\n\t"
    "v_readfirstlane_b32 s53, v30\n\t"
    "s_mov_b32 s44, 0\n\t"
    "s_lshl_b32 s53, s53, 13\n\t"
    "s_add_u32 s45, s53, 16384\n\t"
    "s_add_u32 s46, s53, 1048576\n\t"
    "s_lshl_b32 s55, s52, 13\n\t"
    "s_add_u32 s48, s55, 25165824\n\t"
    "s_add_u32 s49, s55, 41943040\n\t"
    "s_lshl_b32 s55, s52, 12\n\t"
    "s_add_u32 s50, s55, 67108864\n\t"
    "s_cmp_ge_i32 s64, s44\n\t"
    "s_nop 0\n\t"
    "s_cbranch_scc1 HG0d%=\n\t"
    "s_mov_b32 s54, 1000000\n\t"
    "HG0%=:\n\t"
    "buffer_load_dword v60, v15, s[36:39], 0 offen sc1\n\t"
    "s_waitcnt vmcnt(0)\n\t"
    "v_readlane_b32 s55, v60, 0\n\t"
    "v_readlane_b32 s57, v60, 1\n\t"
    "s_min_i32 s64, s55, s57\n\t"
    "s_cmp_ge_i32 s64, s44\n\t"
    "s_nop 0\n\t"
    "s_cbranch_scc1 HG0d%=\n\t"
    "s_sub_u32 s54, s54, 1\n\t"
    "s_cbranch_scc1 HFAIL%=\n\t"
    "s_sleep 1\n\t"
    "s_branch HG0%=\n\t"
    "HG0d%=:\n\t"
    "s_lshl_b32 s57, s51, 11\n\t"
    "s_add_u32 s65, s57, 147456\n\t"
    "s_mov_b32 m0, s65\n\t"
    "s_nop 0\n\t"
    "buffer_load_dwordx4 v1, s[36:39], s48 offen sc1 lds\n\t"
    "buffer_load_dwordx4 v1, s[36:39], s48 offen offset:1024 sc1 lds\n\t"
    "s_add_u32 s65, s57, 155648\n\t"
    "s_mov_b32 m0, s65\n\t"
    "s_nop 0\n\t"
    "buffer_load_dwordx4 v1, s[36:39], s49 offen sc1 lds\n\t"
    "buffer_load_dwordx4 v1, s[36:39], s49 offen offset:1024 sc1 lds\n\t"
    "s_add_u32 s48, s48, 262144\n\t"
    "s_add_u32 s49, s49, 262144\n\t"
    "HFASTD%=:\n\t"
    "s_lshl_b32 s55, s51, 11\n\t"
    "s_add_u32 s68, s55, 147456\n\t"
    "s_add_u32 s69, s55, 155648\n\t"
    "s_mov_b32 s66, 1\n\t"
    "v_mov_b32 v88, 0\n\t"
    "v_mov_b32 v89, 0\n\t"
    "v_mov_b32 v90, 0\n\t"
    "v_mov_b32 v91, 0\n\t"
    "v_mov_b32 v92, 0\n\t"
    "v_mov_b32 v93, 0\n\t"
    "v_mov_b32 v94, 0\n\t"
    "v_mov_b32 v95, 0\n\t"
    "v_mov_b32 v96, 0\n\t"
    "v_mov_b32 v97, 0\n\t"
    "v_mov_b32 v98, 0\n\t"
    "v_mov_b32 v99, 0\n\t"
    "v_mov_b32 v100, 0\n\t"
    "v_mov_b32 v101, 0\n\t"
    "v_mov_b32 v102, 0\n\t"
    "v_mov_b32 v103, 0\n\t"
    "v_mov_b32 v120, 0\n\t"
    "v_mov_b32 v121, 0\n\t"
    "v_mov_b32 v122, 0\n\t"
    "v_mov_b32 v123, 0\n\t"
    "v_mov_b32 v124, 0\n\t"
    "v_mov_b32 v125, 0\n\t"
    "v_mov_b32 v126, 0\n\t"
    "v_mov_b32 v127, 0\n\t"
    "s_waitcnt vmcnt(0)\n\t"
    "s_barrier\n\t"
    "v_add_u32 v65, 147456, v0\n\t"
    "ds_read_b128 v[24:27], v65 offset:0\n\t"
    "ds_read_b128 v[28:31], v65 offset:1024\n\t"
    "ds_read_b128 v[32:35], v65 offset:2048\n\t"
    "ds_read_b128 v[36:39], v65 offset:3072\n\t"
    "ds_read_b128 v[40:43], v65 offset:4096\n\t"
    "ds_read_b128 v[44:47], v65 offset:5120\n\t"
    "ds_read_b128 v[48:51], v65 offset:6144\n\t"
    "ds_read_b128 v[52:55], v65 offset:7168\n\t"
    "ds_read_b128 v[16:19], v10 offset:0\n\t"
    "ds_read_b128 v[20:23], v10 offset:64\n\t"
    "s_waitcnt lgkmcnt(0)\n\t"
    "s_lshl_b32 s55, s51, 0\n\t"
    "s_cmp_eq_u32 s51, 0\n\t"
    "s_cbranch_scc1 HINI0_%=\n\t"
    "s_cmp_eq_u32 s51, 1\n\t"
    "s_cbranch_scc1 HINI1_%=\n\t"
    "s_cmp_eq_u32 s51, 2\n\t"
    "s_cbranch_scc1 HINI2_%=\n\t"
    "s_branch HINI3_%=\n\t"
    "HINI0_%=:\n\t"
    "ds_read_b128 v[56:59], v1\n\t"
    "ds_read_b128 v[60:63], v1 offset:1024\n\t"
    "ds_read_b128 v[104:107], v2 offset:0\n\t"
    "ds_read_b128 v[108:111], v2 offset:1024\n\t"
    "ds_read_b128 v[112:115], v2 offset:8192\n\t"
    "ds_read_b128 v[116:119], v2 offset:9216\n\t"
    "s_waitcnt lgkmcnt(0)\n\t"
    "s_branch HGOT0_%=\n\t"
    "HINI1_%=:\n\t"
    "ds_read_b128 v[64:67], v1\n\t"
    "ds_read_b128 v[68:71], v1 offset:1024\n\t"
    "ds_read_b128 v[104:107], v2 offset:16384\n\t"
    "ds_read_b128 v[108:111], v2 offset:17408\n\t"
    "ds_read_b128 v[112:115], v2 offset:24576\n\t"
    "ds_read_b128 v[116:119], v2 offset:25600\n\t"
    "s_waitcnt lgkmcnt(0)\n\t"
    "s_branch HGOT1_%=\n\t"
    "HINI2_%=:\n\t"
    "ds_read_b128 v[72:75], v1\n\t"
    "ds_read_b128 v[76:79], v1 offset:1024\n\t"
    "ds_read_b128 v[104:107], v2 offset:32768\n\t"
    "ds_read_b128 v[108:111], v2 offset:33792\n\t"
    "ds_read_b128 v[112:115], v2 offset:40960\n\t"
    "ds_read_b128 v[116:119], v2 offset:41984\n\t"
    "s_waitcnt lgkmcnt(0)\n\t"
    "s_branch HGOT2_%=\n\t"
    "HINI3_%=:\n\t"
    "ds_read_b128 v[80:83], v1\n\t"
    "ds_read_b128 v[84:87], v1 offset:1024\n\t"
    "ds_read_b128 v[104:107], v2 offset:49152\n\t"
    "ds_read_b128 v[108:111], v2 offset:50176\n\t"
    "ds_read_b128 v[112:115], v2 offset:57344\n\t"
    "ds_read_b128 v[116:119], v2 offset:58368\n\t"
    "s_waitcnt lgkmcnt(0)\n\t"
    "s_branch HGOT3_%=\n\t"
    "HSTEP0_%=:\n\t"
    "s_mov_b32 s54, 16\n\t"
    "HDL0_%=:\n\t"
    "s_sleep 1\n\t"
    "s_sub_u32 s54, s54, 1\n\t"
    "s_cbranch_scc0 HDL0_%=\n\t"
    "buffer_load_dword v74, v15, s[36:39], 0 offen sc1\n\t"
    "buffer_load_dwordx4 v[56:59], v1, s[36:39], s45 offen sc1\n\t"
    "buffer_load_dwordx4 v[60:63], v1, s[36:39], s45 offen offset:1024 sc1\n\t"
    "s_sleep 1\n\t"
    "s_sleep 1\n\t"
    "buffer_load_dwordx4 v[56:59], v1, s[36:39], s45 offen sc1\n\t"
    "buffer_load_dwordx4 v[60:63], v1, s[36:39], s45 offen offset:1024 sc1\n\t"
    "s_sleep 1\n\t"
    "s_sleep 1\n\t"
    "buffer_load_dwordx4 v[56:59], v1, s[36:39], s45 offen sc1\n\t"
    "buffer_load_dwordx4 v[60:63], v1, s[36:39], s45 offen offset:1024 sc1\n\t"
    "s_sleep 1\n\t"
    "s_sleep 1\n\t"
    "buffer_load_dwordx4 v[56:59], v1, s[36:39], s45 offen sc1\n\t"
    "buffer_load_dwordx4 v[60:63], v1, s[36:39], s45 offen offset:1024 sc1\n\t"
    "s_waitcnt vmcnt(6)\n\t"
    "s_sub_u32 s55, s44, 1\n\t"
    "v_mov_b32 v75, s55\n\t"
    "buffer_store_dword v75, off, s[36:39], s63 sc1\n\t"
    "v_readlane_b32 s55, v74, 0\n\t"
    "v_readlane_b32 s56, v74, 1\n\t"
    "s_min_i32 s64, s55, s56\n\t"
    "s_mov_b32 s54, 1000000\n\t"
    "v_max3_u32 v72, v56, v57, v58\n\t"
    "v_max3_u32 v73, v59, v60, v61\n\t"
    "v_max3_u32 v72, v72, v62, v63\n\t"
    "v_max_u32 v72, v72, v73\n\t"
    "v_cmp_eq_u32 vcc, -1, v72\n\t"
    "s_cbranch_vccz HGOTM0_%=\n\t"
    "HPOLL0_%=:\n\t"
    "buffer_load_dwordx4 v[56:59], v1, s[36:39], s45 offen sc1\n\t"
    "buffer_load_dwordx4 v[60:63], v1, s[36:39], s45 offen offset:1024 sc1\n\t"
    "s_sleep 1\n\t"
    "s_sleep 1\n\t"
    "s_waitcnt vmcnt(6)\n\t"
    "v_max3_u32 v72, v56, v57, v58\n\t"
    "v_max3_u32 v73, v59, v60, v61\n\t"
    "v_max3_u32 v72, v72, v62, v63\n\t"
    "v_max_u32 v72, v72, v73\n\t"
    "v_cmp_eq_u32 vcc, -1, v72\n\t"
    "s_cbranch_vccz HGOTM0_%=\n\t"
    "s_sub_u32 s54, s54, 1\n\t"
    "s_cbranch_scc0 HPOLL0_%=\n\t"
    "s_branch HFAIL%=\n\t"
    "HGOTM0_%=:\n\t"
    "s_add_u32 s45, s45, 16384\n\t"
    "HGOT0_%=:\n\t"
    "ds_write_b128 v1, v[56:59]\n\t"
    "ds_write_b128 v1, v[60:63] offset:1024\n\t"
    "v_smfmac_f32_16x16x64_bf16 v[88:91], v[56:59], a[0:7], v11\n\t"
    "v_smfmac_f32_16x16x64_bf16 v[96:99], v[56:59], a[128:135], v11\n\t"
    "v_smfmac_f32_16x16x64_bf16 v[92:95], v[56:59], a[64:71], v11\n\t"
    "v_smfmac_f32_16x16x64_bf16 v[100:103], v[56:59], v[104:111], v11\n\t"
    "s_waitcnt lgkmcnt(0)\n\t"
    "s_barrier\n\t"
    "ds_read_b128 v[64:67], v0 offset:2048\n\t"
    "ds_read_b128 v[68:71], v0 offset:3072\n\t"
    "ds_read_b128 v[72:75], v0 offset:4096\n\t"
    "ds_read_b128 v[76:79], v0 offset:5120\n\t"
    "ds_read_b128 v[80:83], v0 offset:6144\n\t"
    "ds_read_b128 v[84:87], v0 offset:7168\n\t"
    "ds_read_b128 v[104:107], v2 offset:16384\n\t"
    "ds_read_b128 v[108:111], v2 offset:17408\n\t"
    "v_smfmac_f32_16x16x64_bf16 v[88:91], v[60:63], a[8:15], v11\n\t"
    "v_smfmac_f32_16x16x64_bf16 v[96:99], v[60:63], a[136:143], v11\n\t"
    "v_smfmac_f32_16x16x64_bf16 v[92:95], v[60:63], a[72:79], v11\n\t"
    "v_smfmac_f32_16x16x64_bf16 v[100:103], v[60:63], v[112:119], v11\n\t"
    "ds_read_b128 v[112:115], v2 offset:24576\n\t"
    "ds_read_b128 v[116:119], v2 offset:25600\n\t"
    "v_smfmac_f32_16x16x64_bf16 v[120:123], v[56:59], a[192:199], v11\n\t"
    "v_smfmac_f32_16x16x64_bf16 v[124:127], v[56:59], a[200:207], v11\n\t"
    "v_smfmac_f32_16x16x64_bf16 v[120:123], v[60:63], a[208:215], v11\n\t"
    "v_smfmac_f32_16x16x64_bf16 v[124:127], v[60:63], a[216:223], v11\n\t"
    "s_waitcnt lgkmcnt(9)\n\t"
    "v_smfmac_f32_16x16x64_bf16 v[88:91], v[64:67], a[16:23], v11\n\t"
    "v_smfmac_f32_16x16x64_bf16 v[96:99], v[64:67], a[144:151], v11\n\t"
    "v_smfmac_f32_16x16x64_bf16 v[92:95], v[64:67], a[80:87], v11\n\t"
    "s_waitcnt lgkmcnt(2)\n\t"
    "v_smfmac_f32_16x16x64_bf16 v[100:103], v[64:67], v[104:111], v11\n\t"
    "ds_read_b128 v[104:107], v2 offset:32768\n\t"
    "ds_read_b128 v[108:111], v2 offset:33792\n\t"
    "v_smfmac_f32_16x16x64_bf16 v[88:91], v[68:71], a[24:31], v11\n\t"
    "v_smfmac_f32_16x16x64_bf16 v[96:99], v[68:71], a[152:159], v11\n\t"
    "v_smfmac_f32_16x16x64_bf16 v[92:95], v[68:71], a[88:95], v11\n\t"
    "s_waitcnt lgkmcnt(2)\n\t"
    "v_smfmac_f32_16x16x64_bf16 v[100:103], v[68:71], v[112:119], v11\n\t"
    "ds_read_b128 v[112:115], v2 offset:40960\n\t"
    "ds_read_b128 v[116:119], v2 offset:41984\n\t"
    "v_smfmac_f32_16x16x64_bf16 v[88:91], v[72:75], a[32:39], v11\n\t"
    "v_smfmac_f32_16x16x64_bf16 v[96:99], v[72:75], a[160:167], v11\n\t"
    "v_smfmac_f32_16x16x64_bf16 v[92:95], v[72:75], a[96:103], v11\n\t"
    "s_waitcnt lgkmcnt(2)\n\t"
    "v_smfmac_f32_16x16x64_bf16 v[100:103], v[72:75], v[104:111], v11\n\t"
    "ds_read_b128 v[104:107], v2 offset:49152\n\t"
    "ds_read_b128 v[108:111], v2 offset:50176\n\t"
    "v_smfmac_f32_16x16x64_bf16 v[88:91], v[76:79], a[40:47], v11\n\t"
    "v_smfmac_f32_16x16x64_bf16 v[96:99], v[76:79], a[168:175], v11\n\t"
    "v_smfmac_f32_16x16x64_bf16 v[92:95], v[76:79], a[104:111], v11\n\t"
    "s_waitcnt lgkmcnt(2)\n\t"
    "v_smfmac_f32_16x16x64_bf16 v[100:103], v[76:79], v[112:119], v11\n\t"
    "ds_read_b128 v[112:115], v2 offset:57344\n\t"
    "ds_read_b128 v[116:119], v2 offset:58368\n\t"
    "v_smfmac_f32_16x16x64_bf16 v[88:91], v[80:83], a[48:55], v11\n\t"
    "v_smfmac_f32_16x16x64_bf16 v[96:99], v[80:83], a[176:183], v11\n\t"
    "v_smfmac_f32_16x16x64_bf16 v[92:95], v[80:83], a[112:119], v11\n\t"
    "s_waitcnt lgkmcnt(2)\n\t"
    "v_smfmac_f32_16x16x64_bf16 v[100:103], v[80:83], v[104:111], v11\n\t"
    "v_smfmac_f32_16x16x64_bf16 v[88:91], v[84:87], a[56:63], v11\n\t"
    "v_smfmac_f32_16x16x64_bf16 v[96:99], v[84:87], a[184:191], v11\n\t"
    "v_smfmac_f32_16x16x64_bf16 v[92:95], v[84:87], a[120:127], v11\n\t"
    "s_waitcnt lgkmcnt(0)\n\t"
    "v_smfmac_f32_16x16x64_bf16 v[100:103], v[84:87], v[112:119], v11\n\t"
    "v_smfmac_f32_16x16x64_bf16 v[120:123], v[64:67], a[224:231], v11\n\t"
    "v_smfmac_f32_16x16x64_bf16 v[124:127], v[64:67], a[232:239], v11\n\t"
    "s_nop 2\n\t"
    "v_permlane32_swap_b32 v88, v96\n\t"
    "v_permlane32_swap_b32 v89, v97\n\t"
    "v_permlane32_swap_b32 v90, v98\n\t"
    "v_permlane32_swap_b32 v91, v99\n\t"
    "v_pk_add_f32 v[88:89], v[88:89], v[96:97]\n\t"
    "v_pk_add_f32 v[90:91], v[90:91], v[98:99]\n\t"
    "v_permlane32_swap_b32 v92, v100\n\t"
    "v_permlane32_swap_b32 v93, v101\n\t"
    "v_permlane32_swap_b32 v94, v102\n\t"
    "v_permlane32_swap_b32 v95, v103\n\t"
    "v_pk_add_f32 v[92:93], v[92:93], v[100:101]\n\t"
    "v_pk_add_f32 v[94:95], v[94:95], v[102:103]\n\t"
    "v_cvt_pk_bf16_f32 v96, v88, v92\n\t"
    "v_cvt_pk_bf16_f32 v97, v89, v93\n\t"
    "v_cvt_pk_bf16_f32 v98, v90, v94\n\t"
    "v_cvt_pk_bf16_f32 v99, v91, v95\n\t"
    "ds_write2_b32 v4, v96, v97 offset0:0 offset1:72\n\t"
    "ds_write2_b32 v4, v98, v99 offset0:144 offset1:216\n\t"
    "v_smfmac_f32_16x16x64_bf16 v[120:123], v[68:71], a[240:247], v11\n\t"
    "v_smfmac_f32_16x16x64_bf16 v[124:127], v[68:71], a[248:255], v11\n\t"
    "v_smfmac_f32_16x16x64_bf16 v[120:123], v[72:75], v[192:199], v11\n\t"
    "v_smfmac_f32_16x16x64_bf16 v[124:127], v[72:75], v[200:207], v11\n\t"
    "s_cmp_eq_u32 s44, 63\n\t"
    "s_waitcnt lgkmcnt(0)\n\t"
    "s_barrier\n\t"
    "s_cbranch_scc1 HLAST0_%=\n\t"
    "ds_read_b128 v[88:91], v5 offset:0\n\t"
    "ds_read_b128 v[104:107], v5 offset:2304\n\t"
    "ds_read_b128 v[92:95], v5 offset:64\n\t"
    "ds_read_b128 v[108:111], v5 offset:2368\n\t"
    "ds_read_b128 v[96:99], v5 offset:128\n\t"
    "ds_read_b128 v[112:115], v5 offset:2432\n\t"
    "ds_read_b128 v[100:103], v5 offset:192\n\t"
    "ds_read_b128 v[116:119], v5 offset:2496\n\t"
    "v_smfmac_f32_16x16x64_bf16 v[120:123], v[76:79], v[208:215], v11\n\t"
    "v_smfmac_f32_16x16x64_bf16 v[124:127], v[76:79], v[216:223], v11\n\t"
    "v_smfmac_f32_16x16x64_bf16 v[120:123], v[80:83], v[224:231], v11\n\t"
    "v_smfmac_f32_16x16x64_bf16 v[124:127], v[80:83], v[232:239], v11\n\t"
    "v_smfmac_f32_16x16x64_bf16 v[120:123], v[84:87], v[240:247], v11\n\t"
    "v_smfmac_f32_16x16x64_bf16 v[124:127], v[84:87], v[248:255], v11\n\t"
    "s_branch HQKE%=\n\t"
    "HSTEP1_%=:\n\t"
    "s_mov_b32 s54, 16\n\t"
    "HDL1_%=:\n\t"
    "s_sleep 1\n\t"
    "s_sub_u32 s54, s54, 1\n\t"
    "s_cbranch_scc0 HDL1_%=\n\t"
    "buffer_load_dword v58, v15, s[36:39], 0 offen sc1\n\t"
    "buffer_load_dwordx4 v[64:67], v1, s[36:39], s45 offen sc1\n\t"
    "buffer_load_dwordx4 v[68:71], v1, s[36:39], s45 offen offset:1024 sc1\n\t"
    "s_sleep 1\n\t"
    "s_sleep 1\n\t"
    "buffer_load_dwordx4 v[64:67], v1, s[36:39], s45 offen sc1\n\t"
    "buffer_load_dwordx4 v[68:71], v1, s[36:39], s45 offen offset:1024 sc1\n\t"
    "s_sleep 1\n\t"
    "s_sleep 1\n\t"
    "buffer_load_dwordx4 v[64:67], v1, s[36:39], s45 offen sc1\n\t"
    "buffer_load_dwordx4 v[68:71], v1, s[36:39], s45 offen offset:1024 sc1\n\t"
    "s_sleep 1\n\t"
    "s_sleep 1\n\t"
    "buffer_load_dwordx4 v[64:67], v1, s[36:39], s45 offen sc1\n\t"
    "buffer_load_dwordx4 v[68:71], v1, s[36:39], s45 offen offset:1024 sc1\n\t"
    "s_waitcnt vmcnt(6)\n\t"
    "s_sub_u32 s55, s44, 1\n\t"
    "v_mov_b32 v59, s55\n\t"
    "buffer_store_dword v59, off, s[36:39], s63 sc1\n\t"
    "v_readlane_b32 s55, v58, 0\n\t"
    "v_readlane_b32 s56, v58, 1\n\t"
    "s_min_i32 s64, s55, s56\n\t"
    "s_mov_b32 s54, 1000000\n\t"
    "v_max3_u32 v56, v64, v65, v66\n\t"
    "v_max3_u32 v57, v67, v68, v69\n\t"
    "v_max3_u32 v56, v56, v70, v71\n\t"
    "v_max_u32 v56, v56, v57\n\t"
    "v_cmp_eq_u32 vcc, -1, v56\n\t"
    "s_cbranch_vccz HGOTM1_%=\n\t"
    "HPOLL1_%=:\n\t"
    "buffer_load_dwordx4 v[64:67], v1, s[36:39], s45 offen sc1\n\t"
    "buffer_load_dwordx4 v[68:71], v1, s[36:39], s45 offen offset:1024 sc1\n\t"
    "s_sleep 1\n\t"
    "s_sleep 1\n\t"
    "s_waitcnt vmcnt(6)\n\t"
    "v_max3_u32 v56, v64, v65, v66\n\t"
    "v_max3_u32 v57, v67, v68, v69\n\t"
    "v_max3_u32 v56, v56, v70, v71\n\t"
    "v_max_u32 v56, v56, v57\n\t"
    "v_cmp_eq_u32 vcc, -1, v56\n\t"
    "s_cbranch_vccz HGOTM1_%=\n\t"
    "s_sub_u32 s54, s54, 1\n\t"
    "s_cbranch_scc0 HPOLL1_%=\n\t"
    "s_branch HFAIL%=\n\t"
    "HGOTM1_%=:\n\t"
    "s_add_u32 s45, s45, 16384\n\t"
    "HGOT1_%=:\n\t"
    "ds_write_b128 v1, v[64:67]\n\t"
    "ds_write_b128 v1, v[68:71] offset:1024\n\t"
    "v_smfmac_f32_16x16x64_bf16 v[88:91], v[64:67], a[16:23], v11\n\t"
    "v_smfmac_f32_16x16x64_bf16 v[96:99], v[64:67], a[144:151], v11\n\t"
    "v_smfmac_f32_16x16x64_bf16 v[92:95], v[64:67], a[80:87], v11\n\t"
    "v_smfmac_f32_16x16x64_bf16 v[100:103], v[64:67], v[104:111], v11\n\t"
    "s_waitcnt lgkmcnt(0)\n\t"
    "s_barrier\n\t"
    "ds_read_b128 v[72:75], v0 offset:4096\n\t"
    "ds_read_b128 v[76:79], v0 offset:5120\n\t"
    "ds_read_b128 v[80:83], v0 offset:6144\n\t"
    "ds_read_b128 v[84:87], v0 offset:7168\n\t"
    "ds_read_b128 v[56:59], v0 offset:0\n\t"
    "ds_read_b128 v[60:63], v0 offset:1024\n\t"
    "ds_read_b128 v[104:107], v2 offset:32768\n\t"
    "ds_read_b128 v[108:111], v2 offset:33792\n\t"
    "v_smfmac_f32_16x16x64_bf16 v[88:91], v[68:71], a[24:31], v11\n\t"
    "v_smfmac_f32_16x16x64_bf16 v[96:99], v[68:71], a[152:159], v11\n\t"
    "v_smfmac_f32_16x16x64_bf16 v[92:95], v[68:71], a[88:95], v11\n\t"
    "v_smfmac_f32_16x16x64_bf16 v[100:103], v[68:71], v[112:119], v11\n\t"
    "ds_read_b128 v[112:115], v2 offset:40960\n\t"
    "ds_read_b128 v[116:119], v2 offset:41984\n\t"
    "v_smfmac_f32_16x16x64_bf16 v[120:123], v[64:67], a[224:231], v11\n\t"
    "v_smfmac_f32_16x16x64_bf16 v[124:127], v[64:67], a[232:239], v11\n\t"
    "v_smfmac_f32_16x16x64_bf16 v[120:123], v[68:71], a[240:247], v11\n\t"
    "v_smfmac_f32_16x16x64_bf16 v[124:127], v[68:71], a[248:255], v11\n\t"
    "s_waitcnt lgkmcnt(9)\n\t"
    "v_smfmac_f32_16x16x64_bf16 v[88:91], v[72:75], a[32:39], v11\n\t"
    "v_smfmac_f32_16x16x64_bf16 v[96:99], v[72:75], a[160:167], v11\n\t"
    "v_smfmac_f32_16x16x64_bf16 v[92:95], v[72:75], a[96:103], v11\n\t"
    "s_waitcnt lgkmcnt(2)\n\t"
    "v_smfmac_f32_16x16x64_bf16 v[100:103], v[72:75], v[104:111], v11\n\t"
    "ds_read_b128 v[104:107], v2 offset:49152\n\t"
    "ds_read_b128 v[108:111], v2 offset:50176\n\t"
    "v_smfmac_f32_16x16x64_bf16 v[88:91], v[76:79], a[40:47], v11\n\t"
    "v_smfmac_f32_16x16x64_bf16 v[96:99], v[76:79], a[168:175], v11\n\t"
    "v_smfmac_f32_16x16x64_bf16 v[92:95], v[76:79], a[104:111], v11\n\t"
    "s_waitcnt lgkmcnt(2)\n\t"
    "v_smfmac_f32_16x16x64_bf16 v[100:103], v[76:79], v[112:119], v11\n\t"
    "ds_read_b128 v[112:115], v2 offset:57344\n\t"
    "ds_read_b128 v[116:119], v2 offset:58368\n\t"
    "v_smfmac_f32_16x16x64_bf16 v[88:91], v[80:83], a[48:55], v11\n\t"
    "v_smfmac_f32_16x16x64_bf16 v[96:99], v[80:83], a[176:183], v11\n\t"
    "v_smfmac_f32_16x16x64_bf16 v[92:95], v[80:83], a[112:119], v11\n\t"
    "s_waitcnt lgkmcnt(2)\n\t"
    "v_smfmac_f32_16x16x64_bf16 v[100:103], v[80:83], v[104:111], v11\n\t"
    "ds_read_b128 v[104:107], v2 offset:0\n\t"
    "ds_read_b128 v[108:111], v2 offset:1024\n\t"
    "v_smfmac_f32_16x16x64_bf16 v[88:91], v[84:87], a[56:63], v11\n\t"
    "v_smfmac_f32_16x16x64_bf16 v[96:99], v[84:87], a[184:191], v11\n\t"
    "v_smfmac_f32_16x16x64_bf16 v[92:95], v[84:87], a[120:127], v11\n\t"
    "s_waitcnt lgkmcnt(2)\n\t"
    "v_smfmac_f32_16x16x64_bf16 v[100:103], v[84:87], v[112:119], v11\n\t"
    "ds_read_b128 v[112:115], v2 offset:8192\n\t"
    "ds_read_b128 v[116:119], v2 offset:9216\n\t"
    "v_smfmac_f32_16x16x64_bf16 v[88:91], v[56:59], a[0:7], v11\n\t"
    "v_smfmac_f32_16x16x64_bf16 v[96:99], v[56:59], a[128:135], v11\n\t"
    "v_smfmac_f32_16x16x64_bf16 v[92:95], v[56:59], a[64:71], v11\n\t"
    "s_waitcnt lgkmcnt(2)\n\t"
    "v_smfmac_f32_16x16x64_bf16 v[100:103], v[56:59], v[104:111], v11\n\t"
    "v_smfmac_f32_16x16x64_bf16 v[88:91], v[60:63], a[8:15], v11\n\t"
    "v_smfmac_f32_16x16x64_bf16 v[96:99], v[60:63], a[136:143], v11\n\t"
    "v_smfmac_f32_16x16x64_bf16 v[92:95], v[60:63], a[72:79], v11\n\t"
    "s_waitcnt lgkmcnt(0)\n\t"
    "v_smfmac_f32_16x16x64_bf16 v[100:103], v[60:63], v[112:119], v11\n\t"
    "v_smfmac_f32_16x16x64_bf16 v[120:123], v[72:75], v[192:199], v11\n\t"
    "v_smfmac_f32_16x16x64_bf16 v[124:127], v[72:75], v[200:207], v11\n\t"
    "s_nop 2\n\t"
    "v_permlane32_swap_b32 v88, v96\n\t"
    "v_permlane32_swap_b32 v89, v97\n\t"
    "v_permlane32_swap_b32 v90, v98\n\t"
    "v_permlane32_swap_b32 v91, v99\n\t"
    "v_pk_add_f32 v[88:89], v[88:89], v[96:97]\n\t"
    "v_pk_add_f32 v[90:91], v[90:91], v[98:99]\n\t"
    "v_permlane32_swap_b32 v92, v100\n\t"
    "v_permlane32_swap_b32 v93, v101\n\t"
    "v_permlane32_swap_b32 v94, v102\n\t"
    "v_permlane32_swap_b32 v95, v103\n\t"
    "v_pk_add_f32 v[92:93], v[92:93], v[100:101]\n\t"
    "v_pk_add_f32 v[94:95], v[94:95], v[102:103]\n\t"
    "v_cvt_pk_bf16_f32 v96, v88, v92\n\t"
    "v_cvt_pk_bf16_f32 v97, v89, v93\n\t"
    "v_cvt_pk_bf16_f32 v98, v90, v94\n\t"
    "v_cvt_pk_bf16_f32 v99, v91, v95\n\t"
    "ds_write2_b32 v4, v96, v97 offset0:0 offset1:72\n\t"
    "ds_write2_b32 v4, v98, v99 offset0:144 offset1:216\n\t"
    "v_smfmac_f32_16x16x64_bf16 v[120:123], v[76:79], v[208:215], v11\n\t"
    "v_smfmac_f32_16x16x64_bf16 v[124:127], v[76:79], v[216:223], v11\n\t"
    "v_smfmac_f32_16x16x64_bf16 v[120:123], v[80:83], v[224:231], v11\n\t"
    "v_smfmac_f32_16x16x64_bf16 v[124:127], v[80:83], v[232:239], v11\n\t"
    "s_cmp_eq_u32 s44, 63\n\t"
    "s_waitcnt lgkmcnt(0)\n\t"
    "s_barrier\n\t"
    "s_cbranch_scc1 HLAST1_%=\n\t"
    "ds_read_b128 v[88:91], v5 offset:0\n\t"
    "ds_read_b128 v[104:107], v5 offset:2304\n\t"
    "ds_read_b128 v[92:95], v5 offset:64\n\t"
    "ds_read_b128 v[108:111], v5 offset:2368\n\t"
    "ds_read_b128 v[96:99], v5 offset:128\n\t"
    "ds_read_b128 v[112:115], v5 offset:2432\n\t"
    "ds_read_b128 v[100:103], v5 offset:192\n\t"
    "ds_read_b128 v[116:119], v5 offset:2496\n\t"
    "v_smfmac_f32_16x16x64_bf16 v[120:123], v[84:87], v[240:247], v11\n\t"
    "v_smfmac_f32_16x16x64_bf16 v[124:127], v[84:87], v[248:255], v11\n\t"
    "v_smfmac_f32_16x16x64_bf16 v[120:123], v[56:59], a[192:199], v11\n\t"
    "v_smfmac_f32_16x16x64_bf16 v[124:127], v[56:59], a[200:207], v11\n\t"
    "v_smfmac_f32_16x16x64_bf16 v[120:123], v[60:63], a[208:215], v11\n\t"
    "v_smfmac_f32_16x16x64_bf16 v[124:127], v[60:63], a[216:223], v11\n\t"
    "s_branch HQKE%=\n\t"
    "HSTEP2_%=:\n\t"
    "s_mov_b32 s54, 16\n\t"
    "HDL2_%=:\n\t"
    "s_sleep 1\n\t"
    "s_sub_u32 s54, s54, 1\n\t"
    "s_cbranch_scc0 HDL2_%=\n\t"
    "buffer_load_dword v58, v15, s[36:39], 0 offen sc1\n\t"
    "buffer_load_dwordx4 v[72:75], v1, s[36:39], s45 offen sc1\n\t"
    "buffer_load_dwordx4 v[76:79], v1, s[36:39], s45 offen offset:1024 sc1\n\t"
    "s_sleep 1\n\t"
    "s_sleep 1\n\t"
    "buffer_load_dwordx4 v[72:75], v1, s[36:39], s45 offen sc1\n\t"
    "buffer_load_dwordx4 v[76:79], v1, s[36:39], s45 offen offset:1024 sc1\n\t"
    "s_sleep 1\n\t"
    "s_sleep 1\n\t"
    "buffer_load_dwordx4 v[72:75], v1, s[36:39], s45 offen sc1\n\t"
    "buffer_load_dwordx4 v[76:79], v1, s[36:39], s45 offen offset:1024 sc1\n\t"
    "s_sleep 1\n\t"
    "s_sleep 1\n\t"
    "buffer_load_dwordx4 v[72:75], v1, s[36:39], s45 offen sc1\n\t"
    "buffer_load_dwordx4 v[76:79], v1, s[36:39], s45 offen offset:1024 sc1\n\t"
    "s_waitcnt vmcnt(6)\n\t"
    "s_sub_u32 s55, s44, 1\n\t"
    "v_mov_b32 v59, s55\n\t"
    "buffer_store_dword v59, off, s[36:39], s63 sc1\n\t"
    "v_readlane_b32 s55, v58, 0\n\t"
    "v_readlane_b32 s56, v58, 1\n\t"
    "s_min_i32 s64, s55, s56\n\t"
    "s_mov_b32 s54, 1000000\n\t"
    "v_max3_u32 v56, v72, v73, v74\n\t"
    "v_max3_u32 v57, v75, v76, v77\n\t"
    "v_max3_u32 v56, v56, v78, v79\n\t"
    "v_max_u32 v56, v56, v57\n\t"
    "v_cmp_eq_u32 vcc, -1, v56\n\t"
    "s_cbranch_vccz HGOTM2_%=\n\t"
    "HPOLL2_%=:\n\t"
    "buffer_load_dwordx4 v[72:75], v1, s[36:39], s45 offen sc1\n\t"
    "buffer_load_dwordx4 v[76:79], v1, s[36:39], s45 offen offset:1024 sc1\n\t"
    "s_sleep 1\n\t"
    "s_sleep 1\n\t"
    "s_waitcnt vmcnt(6)\n\t"
    "v_max3_u32 v56, v72, v73, v74\n\t"
    "v_max3_u32 v57, v75, v76, v77\n\t"
    "v_max3_u32 v56, v56, v78, v79\n\t"
    "v_max_u32 v56, v56, v57\n\t"
    "v_cmp_eq_u32 vcc, -1, v56\n\t"
    "s_cbranch_vccz HGOTM2_%=\n\t"
    "s_sub_u32 s54, s54, 1\n\t"
    "s_cbranch_scc0 HPOLL2_%=\n\t"
    "s_branch HFAIL%=\n\t"
    "HGOTM2_%=:\n\t"
    "s_add_u32 s45, s45, 16384\n\t"
    "HGOT2_%=:\n\t"
    "ds_write_b128 v1, v[72:75]\n\t"
    "ds_write_b128 v1, v[76:79] offset:1024\n\t"
    "v_smfmac_f32_16x16x64_bf16 v[88:91], v[72:75], a[32:39], v11\n\t"
    "v_smfmac_f32_16x16x64_bf16 v[96:99], v[72:75], a[160:167], v11\n\t"
    "v_smfmac_f32_16x16x64_bf16 v[92:95], v[72:75], a[96:103], v11\n\t"
    "v_smfmac_f32_16x16x64_bf16 v[100:103], v[72:75], v[104:111], v11\n\t"
    "s_waitcnt lgkmcnt(0)\n\t"
    "s_barrier\n\t"
    "ds_read_b128 v[80:83], v0 offset:6144\n\t"
    "ds_read_b128 v[84:87], v0 offset:7168\n\t"
    "ds_read_b128 v[56:59], v0 offset:0\n\t"
    "ds_read_b128 v[60:63], v0 offset:1024\n\t"
    "ds_read_b128 v[64:67], v0 offset:2048\n\t"
    "ds_read_b128 v[68:71], v0 offset:3072\n\t"
    "ds_read_b128 v[104:107], v2 offset:49152\n\t"
    "ds_read_b128 v[108:111], v2 offset:50176\n\t"
    "v_smfmac_f32_16x16x64_bf16 v[88:91], v[76:79], a[40:47], v11\n\t"
    "v_smfmac_f32_16x16x64_bf16 v[96:99], v[76:79], a[168:175], v11\n\t"
    "v_smfmac_f32_16x16x64_bf16 v[92:95], v[76:79], a[104:111], v11\n\t"
    "v_smfmac_f32_16x16x64_bf16 v[100:103], v[76:79], v[112:119], v11\n\t"
    "ds_read_b128 v[112:115], v2 offset:57344\n\t"
    "ds_read_b128 v[116:119], v2 offset:58368\n\t"
    "v_smfmac_f32_16x16x64_bf16 v[120:123], v[72:75], v[192:199], v11\n\t"
    "v_smfmac_f32_16x16x64_bf16 v[124:127], v[72:75], v[200:207], v11\n\t"
    "v_smfmac_f32_16x16x64_bf16 v[120:123], v[76:79], v[208:215], v11\n\t"
    "v_smfmac_f32_16x16x64_bf16 v[124:127], v[76:79], v[216:223], v11\n\t"
    "s_waitcnt lgkmcnt(9)\n\t"
    "v_smfmac_f32_16x16x64_bf16 v[88:91], v[80:83], a[48:55], v11\n\t"
    "v_smfmac_f32_16x16x64_bf16 v[96:99], v[80:83], a[176:183], v11\n\t"
    "v_smfmac_f32_16x16x64_bf16 v[92:95], v[80:83], a[112:119], v11\n\t"
    "s_waitcnt lgkmcnt(2)\n\t"
    "v_smfmac_f32_16x16x64_bf16 v[100:103], v[80:83], v[104:111], v11\n\t"
    "ds_read_b128 v[104:107], v2 offset:0\n\t"
    "ds_read_b128 v[108:111], v2 offset:1024\n\t"
    "v_smfmac_f32_16x16x64_bf16 v[88:91], v[84:87], a[56:63], v11\n\t"
    "v_smfmac_f32_16x16x64_bf16 v[96:99], v[84:87], a[184:191], v11\n\t"
    "v_smfmac_f32_16x16x64_bf16 v[92:95], v[84:87], a[120:127], v11\n\t"
    "s_waitcnt lgkmcnt(2)\n\t"
    "v_smfmac_f32_16x16x64_bf16 v[100:103], v[84:87], v[112:119], v11\n\t"
    "ds_read_b128 v[112:115], v2 offset:8192\n\t"
    "ds_read_b128 v[116:119], v2 offset:9216\n\t"
    "v_smfmac_f32_16x16x64_bf16 v[88:91], v[56:59], a[0:7], v11\n\t"
    "v_smfmac_f32_16x16x64_bf16 v[96:99], v[56:59], a[128:135], v11\n\t"
    "v_smfmac_f32_16x16x64_bf16 v[92:95], v[56:59], a[64:71], v11\n\t"
    "s_waitcnt lgkmcnt(2)\n\t"
    "v_smfmac_f32_16x16x64_bf16 v[100:103], v[56:59], v[104:111], v11\n\t"
    "ds_read_b128 v[104:107], v2 offset:16384\n\t"
    "ds_read_b128 v[108:111], v2 offset:17408\n\t"
    "v_smfmac_f32_16x16x64_bf16 v[88:91], v[60:63], a[8:15], v11\n\t"
    "v_smfmac_f32_16x16x64_bf16 v[96:99], v[60:63], a[136:143], v11\n\t"
    "v_smfmac_f32_16x16x64_bf16 v[92:95], v[60:63], a[72:79], v11\n\t"
    "s_waitcnt lgkmcnt(2)\n\t"
    "v_smfmac_f32_16x16x64_bf16 v[100:103], v[60:63], v[112:119], v11\n\t"
    "ds_read_b128 v[112:115], v2 offset:24576\n\t"
    "ds_read_b128 v[116:119], v2 offset:25600\n\t"
    "v_smfmac_f32_16x16x64_bf16 v[88:91], v[64:67], a[16:23], v11\n\t"
    "v_smfmac_f32_16x16x64_bf16 v[96:99], v[64:67], a[144:151], v11\n\t"
    "v_smfmac_f32_16x16x64_bf16 v[92:95], v[64:67], a[80:87], v11\n\t"
    "s_waitcnt lgkmcnt(2)\n\t"
    "v_smfmac_f32_16x16x64_bf16 v[100:103], v[64:67], v[104:111], v11\n\t"
    "v_smfmac_f32_16x16x64_bf16 v[88:91], v[68:71], a[24:31], v11\n\t"
    "v_smfmac_f32_16x16x64_bf16 v[96:99], v[68:71], a[152:159], v11\n\t"
    "v_smfmac_f32_16x16x64_bf16 v[92:95], v[68:71], a[88:95], v11\n\t"
    "s_waitcnt lgkmcnt(0)\n\t"
    "v_smfmac_f32_16x16x64_bf16 v[100:103], v[68:71], v[112:119], v11\n\t"
    "v_smfmac_f32_16x16x64_bf16 v[120:123], v[80:83], v[224:231], v11\n\t"
    "v_smfmac_f32_16x16x64_bf16 v[124:127], v[80:83], v[232:239], v11\n\t"
    "s_nop 2\n\t"
    "v_permlane32_swap_b32 v88, v96\n\t"
    "v_permlane32_swap_b32 v89, v97\n\t"
    "v_permlane32_swap_b32 v90, v98\n\t"
    "v_permlane32_swap_b32 v91, v99\n\t"
    "v_pk_add_f32 v[88:89], v[88:89], v[96:97]\n\t"
    "v_pk_add_f32 v[90:91], v[90:91], v[98:99]\n\t"
    "v_permlane32_swap_b32 v92, v100\n\t"
    "v_permlane32_swap_b32 v93, v101\n\t"
    "v_permlane32_swap_b32 v94, v102\n\t"
    "v_permlane32_swap_b32 v95, v103\n\t"
    "v_pk_add_f32 v[92:93], v[92:93], v[100:101]\n\t"
    "v_pk_add_f32 v[94:95], v[94:95], v[102:103]\n\t"
    "v_cvt_pk_bf16_f32 v96, v88, v92\n\t"
    "v_cvt_pk_bf16_f32 v97, v89, v93\n\t"
    "v_cvt_pk_bf16_f32 v98, v90, v94\n\t"
    "v_cvt_pk_bf16_f32 v99, v91, v95\n\t"
    "ds_write2_b32 v4, v96, v97 offset0:0 offset1:72\n\t"
    "ds_write2_b32 v4, v98, v99 offset0:144 offset1:216\n\t"
    "v_smfmac_f32_16x16x64_bf16 v[120:123], v[84:87], v[240:247], v11\n\t"
    "v_smfmac_f32_16x16x64_bf16 v[124:127], v[84:87], v[248:255], v11\n\t"
    "v_smfmac_f32_16x16x64_bf16 v[120:123], v[56:59], a[192:199], v11\n\t"
    "v_smfmac_f32_16x16x64_bf16 v[124:127], v[56:59], a[200:207], v11\n\t"
    "s_cmp_eq_u32 s44, 63\n\t"
    "s_waitcnt lgkmcnt(0)\n\t"
    "s_barrier\n\t"
    "s_cbranch_scc1 HLAST2_%=\n\t"
    "ds_read_b128 v[88:91], v5 offset:0\n\t"
    "ds_read_b128 v[104:107], v5 offset:2304\n\t"
    "ds_read_b128 v[92:95], v5 offset:64\n\t"
    "ds_read_b128 v[108:111], v5 offset:2368\n\t"
    "ds_read_b128 v[96:99], v5 offset:128\n\t"
    "ds_read_b128 v[112:115], v5 offset:2432\n\t"
    "ds_read_b128 v[100:103], v5 offset:192\n\t"
    "ds_read_b128 v[116:119], v5 offset:2496\n\t"
    "v_smfmac_f32_16x16x64_bf16 v[120:123], v[60:63], a[208:215], v11\n\t"
    "v_smfmac_f32_16x16x64_bf16 v[124:127], v[60:63], a[216:223], v11\n\t"
    "v_smfmac_f32_16x16x64_bf16 v[120:123], v[64:67], a[224:231], v11\n\t"
    "v_smfmac_f32_16x16x64_bf16 v[124:127], v[64:67], a[232:239], v11\n\t"
    "v_smfmac_f32_16x16x64_bf16 v[120:123], v[68:71], a[240:247], v11\n\t"
    "v_smfmac_f32_16x16x64_bf16 v[124:127], v[68:71], a[248:255], v11\n\t"
    "s_branch HQKE%=\n\t"
    "HSTEP3_%=:\n\t"
    "s_mov_b32 s54, 16\n\t"
    "HDL3_%=:\n\t"
    "s_sleep 1\n\t"
    "s_sub_u32 s54, s54, 1\n\t"
    "s_cbranch_scc0 HDL3_%=\n\t"
    "buffer_load_dword v58, v15, s[36:39], 0 offen sc1\n\t"
    "buffer_load_dwordx4 v[80:83], v1, s[36:39], s45 offen sc1\n\t"
    "buffer_load_dwordx4 v[84:87], v1, s[36:39], s45 offen offset:1024 sc1\n\t"
    "s_sleep 1\n\t"
    "s_sleep 1\n\t"
    "buffer_load_dwordx4 v[80:83], v1, s[36:39], s45 offen sc1\n\t"
    "buffer_load_dwordx4 v[84:87], v1, s[36:39], s45 offen offset:1024 sc1\n\t"
    "s_sleep 1\n\t"
    "s_sleep 1\n\t"
    "buffer_load_dwordx4 v[80:83], v1, s[36:39], s45 offen sc1\n\t"
    "buffer_load_dwordx4 v[84:87], v1, s[36:39], s45 offen offset:1024 sc1\n\t"
    "s_sleep 1\n\t"
    "s_sleep 1\n\t"
    "buffer_load_dwordx4 v[80:83], v1, s[36:39], s45 offen sc1\n\t"
    "buffer_load_dwordx4 v[84:87], v1, s[36:39], s45 offen offset:1024 sc1\n\t"
    "s_waitcnt vmcnt(6)\n\t"
    "s_sub_u32 s55, s44, 1\n\t"
    "v_mov_b32 v59, s55\n\t"
    "buffer_store_dword v59, off, s[36:39], s63 sc1\n\t"
    "v_readlane_b32 s55, v58, 0\n\t"
    "v_readlane_b32 s56, v58, 1\n\t"
    "s_min_i32 s64, s55, s56\n\t"
    "s_mov_b32 s54, 1000000\n\t"
    "v_max3_u32 v56, v80, v81, v82\n\t"
    "v_max3_u32 v57, v83, v84, v85\n\t"
    "v_max3_u32 v56, v56, v86, v87\n\t"
    "v_max_u32 v56, v56, v57\n\t"
    "v_cmp_eq_u32 vcc, -1, v56\n\t"
    "s_cbranch_vccz HGOTM3_%=\n\t"
    "HPOLL3_%=:\n\t"
    "buffer_load_dwordx4 v[80:83], v1, s[36:39], s45 offen sc1\n\t"
    "buffer_load_dwordx4 v[84:87], v1, s[36:39], s45 offen offset:1024 sc1\n\t"
    "s_sleep 1\n\t"
    "s_sleep 1\n\t"
    "s_waitcnt vmcnt(6)\n\t"
    "v_max3_u32 v56, v80, v81, v82\n\t"
    "v_max3_u32 v57, v83, v84, v85\n\t"
    "v_max3_u32 v56, v56, v86, v87\n\t"
    "v_max_u32 v56, v56, v57\n\t"
    "v_cmp_eq_u32 vcc, -1, v56\n\t"
    "s_cbranch_vccz HGOTM3_%=\n\t"
    "s_sub_u32 s54, s54, 1\n\t"
    "s_cbranch_scc0 HPOLL3_%=\n\t"
    "s_branch HFAIL%=\n\t"
    "HGOTM3_%=:\n\t"
    "s_add_u32 s45, s45, 16384\n\t"
    "HGOT3_%=:\n\t"
    "ds_write_b128 v1, v[80:83]\n\t"
    "ds_write_b128 v1, v[84:87] offset:1024\n\t"
    "v_smfmac_f32_16x16x64_bf16 v[88:91], v[80:83], a[48:55], v11\n\t"
    "v_smfmac_f32_16x16x64_bf16 v[96:99], v[80:83], a[176:183], v11\n\t"
    "v_smfmac_f32_16x16x64_bf16 v[92:95], v[80:83], a[112:119], v11\n\t"
    "v_smfmac_f32_16x16x64_bf16 v[100:103], v[80:83], v[104:111], v11\n\t"
    "s_waitcnt lgkmcnt(0)\n\t"
    "s_barrier\n\t"
    "ds_read_b128 v[56:59], v0 offset:0\n\t"
    "ds_read_b128 v[60:63], v0 offset:1024\n\t"
    "ds_read_b128 v[64:67], v0 offset:2048\n\t"
    "ds_read_b128 v[68:71], v0 offset:3072\n\t"
    "ds_read_b128 v[72:75], v0 offset:4096\n\t"
    "ds_read_b128 v[76:79], v0 offset:5120\n\t"
    "ds_read_b128 v[104:107], v2 offset:0\n\t"
    "ds_read_b128 v[108:111], v2 offset:1024\n\t"
    "v_smfmac_f32_16x16x64_bf16 v[88:91], v[84:87], a[56:63], v11\n\t"
    "v_smfmac_f32_16x16x64_bf16 v[96:99], v[84:87], a[184:191], v11\n\t"
    "v_smfmac_f32_16x16x64_bf16 v[92:95], v[84:87], a[120:127], v11\n\t"
    "v_smfmac_f32_16x16x64_bf16 v[100:103], v[84:87], v[112:119], v11\n\t"
    "ds_read_b128 v[112:115], v2 offset:8192\n\t"
    "ds_read_b128 v[116:119], v2 offset:9216\n\t"
    "v_smfmac_f32_16x16x64_bf16 v[120:123], v[80:83], v[224:231], v11\n\t"
    "v_smfmac_f32_16x16x64_bf16 v[124:127], v[80:83], v[232:239], v11\n\t"
    "v_smfmac_f32_16x16x64_bf16 v[120:123], v[84:87], v[240:247], v11\n\t"
    "v_smfmac_f32_16x16x64_bf16 v[124:127], v[84:87], v[248:255], v11\n\t"
    "s_waitcnt lgkmcnt(9)\n\t"
    "v_smfmac_f32_16x16x64_bf16 v[88:91], v[56:59], a[0:7], v11\n\t"
    "v_smfmac_f32_16x16x64_bf16 v[96:99], v[56:59], a[128:135], v11\n\t"
    "v_smfmac_f32_16x16x64_bf16 v[92:95], v[56:59], a[64:71], v11\n\t"
    "s_waitcnt lgkmcnt(2)\n\t"
    "v_smfmac_f32_16x16x64_bf16 v[100:103], v[56:59], v[104:111], v11\n\t"
    "ds_read_b128 v[104:107], v2 offset:16384\n\t"
    "ds_read_b128 v[108:111], v2 offset:17408\n\t"
    "v_smfmac_f32_16x16x64_bf16 v[88:91], v[60:63], a[8:15], v11\n\t"
    "v_smfmac_f32_16x16x64_bf16 v[96:99], v[60:63], a[136:143], v11\n\t"
    "v_smfmac_f32_16x16x64_bf16 v[92:95], v[60:63], a[72:79], v11\n\t"
    "s_waitcnt lgkmcnt(2)\n\t"
    "v_smfmac_f32_16x16x64_bf16 v[100:103], v[60:63], v[112:119], v11\n\t"
    "ds_read_b128 v[112:115], v2 offset:24576\n\t"
    "ds_read_b128 v[116:119], v2 offset:25600\n\t"
    "v_smfmac_f32_16x16x64_bf16 v[88:91], v[64:67], a[16:23], v11\n\t"
    "v_smfmac_f32_16x16x64_bf16 v[96:99], v[64:67], a[144:151], v11\n\t"
    "v_smfmac_f32_16x16x64_bf16 v[92:95], v[64:67], a[80:87], v11\n\t"
    "s_waitcnt lgkmcnt(2)\n\t"
    "v_smfmac_f32_16x16x64_bf16 v[100:103], v[64:67], v[104:111], v11\n\t"
    "ds_read_b128 v[104:107], v2 offset:32768\n\t"
    "ds_read_b128 v[108:111], v2 offset:33792\n\t"
    "v_smfmac_f32_16x16x64_bf16 v[88:91], v[68:71], a[24:31], v11\n\t"
    "v_smfmac_f32_16x16x64_bf16 v[96:99], v[68:71], a[152:159], v11\n\t"
    "v_smfmac_f32_16x16x64_bf16 v[92:95], v[68:71], a[88:95], v11\n\t"
    "s_waitcnt lgkmcnt(2)\n\t"
    "v_smfmac_f32_16x16x64_bf16 v[100:103], v[68:71], v[112:119], v11\n\t"
    "ds_read_b128 v[112:115], v2 offset:40960\n\t"
    "ds_read_b128 v[116:119], v2 offset:41984\n\t"
    "v_smfmac_f32_16x16x64_bf16 v[88:91], v[72:75], a[32:39], v11\n\t"
    "v_smfmac_f32_16x16x64_bf16 v[96:99], v[72:75], a[160:167], v11\n\t"
    "v_smfmac_f32_16x16x64_bf16 v[92:95], v[72:75], a[96:103], v11\n\t"
    "s_waitcnt lgkmcnt(2)\n\t"
    "v_smfmac_f32_16x16x64_bf16 v[100:103], v[72:75], v[104:111], v11\n\t"
    "v_smfmac_f32_16x16x64_bf16 v[88:91], v[76:79], a[40:47], v11\n\t"
    "v_smfmac_f32_16x16x64_bf16 v[96:99], v[76:79], a[168:175], v11\n\t"
    "v_smfmac_f32_16x16x64_bf16 v[92:95], v[76:79], a[104:111], v11\n\t"
    "s_waitcnt lgkmcnt(0)\n\t"
    "v_smfmac_f32_16x16x64_bf16 v[100:103], v[76:79], v[112:119], v11\n\t"
    "v_smfmac_f32_16x16x64_bf16 v[120:123], v[56:59], a[192:199], v11\n\t"
    "v_smfmac_f32_16x16x64_bf16 v[124:127], v[56:59], a[200:207], v11\n\t"
    "s_nop 2\n\t"
    "v_permlane32_swap_b32 v88, v96\n\t"
    "v_permlane32_swap_b32 v89, v97\n\t"
    "v_permlane32_swap_b32 v90, v98\n\t"
    "v_permlane32_swap_b32 v91, v99\n\t"
    "v_pk_add_f32 v[88:89], v[88:89], v[96:97]\n\t"
    "v_pk_add_f32 v[90:91], v[90:91], v[98:99]\n\t"
    "v_permlane32_swap_b32 v92, v100\n\t"
    "v_permlane32_swap_b32 v93, v101\n\t"
    "v_permlane32_swap_b32 v94, v102\n\t"
    "v_permlane32_swap_b32 v95, v103\n\t"
    "v_pk_add_f32 v[92:93], v[92:93], v[100:101]\n\t"
    "v_pk_add_f32 v[94:95], v[94:95], v[102:103]\n\t"
    "v_cvt_pk_bf16_f32 v96, v88, v92\n\t"
    "v_cvt_pk_bf16_f32 v97, v89, v93\n\t"
    "v_cvt_pk_bf16_f32 v98, v90, v94\n\t"
    "v_cvt_pk_bf16_f32 v99, v91, v95\n\t"
    "ds_write2_b32 v4, v96, v97 offset0:0 offset1:72\n\t"
    "ds_write2_b32 v4, v98, v99 offset0:144 offset1:216\n\t"
    "v_smfmac_f32_16x16x64_bf16 v[120:123], v[60:63], a[208:215], v11\n\t"
    "v_smfmac_f32_16x16x64_bf16 v[124:127], v[60:63], a[216:223], v11\n\t"
    "v_smfmac_f32_16x16x64_bf16 v[120:123], v[64:67], a[224:231], v11\n\t"
    "v_smfmac_f32_16x16x64_bf16 v[124:127], v[64:67], a[232:239], v11\n\t"
    "s_cmp_eq_u32 s44, 63\n\t"
    "s_waitcnt lgkmcnt(0)\n\t"
    "s_barrier\n\t"
    "s_cbranch_scc1 HLAST3_%=\n\t"
    "ds_read_b128 v[88:91], v5 offset:0\n\t"
    "ds_read_b128 v[104:107], v5 offset:2304\n\t"
    "ds_read_b128 v[92:95], v5 offset:64\n\t"
    "ds_read_b128 v[108:111], v5 offset:2368\n\t"
    "ds_read_b128 v[96:99], v5 offset:128\n\t"
    "ds_read_b128 v[112:115], v5 offset:2432\n\t"
    "ds_read_b128 v[100:103], v5 offset:192\n\t"
    "ds_read_b128 v[116:119], v5 offset:2496\n\t"
    "v_smfmac_f32_16x16x64_bf16 v[120:123], v[68:71], a[240:247], v11\n\t"
    "v_smfmac_f32_16x16x64_bf16 v[124:127], v[68:71], a[248:255], v11\n\t"
    "v_smfmac_f32_16x16x64_bf16 v[120:123], v[72:75], v[192:199], v11\n\t"
    "v_smfmac_f32_16x16x64_bf16 v[124:127], v[72:75], v[200:207], v11\n\t"
    "v_smfmac_f32_16x16x64_bf16 v[120:123], v[76:79], v[208:215], v11\n\t"
    "v_smfmac_f32_16x16x64_bf16 v[124:127], v[76:79], v[216:223], v11\n\t"
    "HQKE%=:\n\t"
    "s_waitcnt vmcnt(0)\n\t"
    "s_waitcnt lgkmcnt(7)\n\t"
    "v_mfma_f32_16x16x32_bf16 v[56:59], v[24:27], v[88:91], 0\n\t"
    "v_mfma_f32_16x16x32_bf16 v[60:63], v[40:43], v[88:91], 0\n\t"
    "s_waitcnt lgkmcnt(6)\n\t"
    "v_mfma_f32_16x16x32_bf16 v[64:67], v[104:107], v[88:91], 0\n\t"
    "s_waitcnt lgkmcnt(5)\n\t"
    "v_mfma_f32_16x16x32_bf16 v[56:59], v[28:31], v[92:95], v[56:59]\n\t"
    "v_mfma_f32_16x16x32_bf16 v[60:63], v[44:47], v[92:95], v[60:63]\n\t"
    "s_waitcnt lgkmcnt(4)\n\t"
    "v_mfma_f32_16x16x32_bf16 v[64:67], v[108:111], v[92:95], v[64:67]\n\t"
    "s_waitcnt lgkmcnt(3)\n\t"
    "v_mfma_f32_16x16x32_bf16 v[56:59], v[32:35], v[96:99], v[56:59]\n\t"
    "v_mfma_f32_16x16x32_bf16 v[60:63], v[48:51], v[96:99], v[60:63]\n\t"
    "s_waitcnt lgkmcnt(2)\n\t"
    "v_mfma_f32_16x16x32_bf16 v[64:67], v[112:115], v[96:99], v[64:67]\n\t"
    "ds_read_b128 v[24:27], v3 offset:0\n\t"
    "ds_read_b128 v[28:31], v3 offset:1024\n\t"
    "ds_read_b128 v[88:91], v3 offset:16384\n\t"
    "ds_read_b128 v[92:95], v3 offset:17408\n\t"
    "s_waitcnt lgkmcnt(5)\n\t"
    "v_mfma_f32_16x16x32_bf16 v[56:59], v[36:39], v[100:103], v[56:59]\n\t"
    "v_mfma_f32_16x16x32_bf16 v[60:63], v[52:55], v[100:103], v[60:63]\n\t"
    "s_waitcnt lgkmcnt(4)\n\t"
    "v_mfma_f32_16x16x32_bf16 v[64:67], v[116:119], v[100:103], v[64:67]\n\t"
    "ds_read_b128 v[40:43], v3 offset:8192\n\t"
    "ds_read_b128 v[44:47], v3 offset:9216\n\t"
    "ds_read_b128 v[104:107], v3 offset:24576\n\t"
    "ds_read_b128 v[108:111], v3 offset:25600\n\t"
    "s_nop 0\n\t"
    "v_max3_f32 v68, v56, v57, v58\n\t"
    "v_max3_f32 v69, v59, v60, v61\n\t"
    "v_max3_f32 v68, v68, v62, v63\n\t"
    "v_max3_f32 v69, v69, v64, v65\n\t"
    "v_max3_f32 v68, v68, v66, v67\n\t"
    "v_max_f32 v68, v68, v69\n\t"
    "v_mov_b32 v69, v68\n\t"
    "v_mov_b32 v84, 0x3f803f80\n\t"
    "v_mov_b32 v85, 0x3f803f80\n\t"
    "v_permlane32_swap_b32 v68, v69\n\t"
    "v_max_f32 v68, v68, v69\n\t"
    "v_mov_b32 v69, v68\n\t"
    "v_mov_b32 v86, 0x3f803f80\n\t"
    "v_mov_b32 v87, 0x3f803f80\n\t"
    "v_permlane16_swap_b32 v68, v69\n\t"
    "v_max_f32 v68, v68, v69\n\t"
    "v_mul_f32 v68, s60, v68\n\t"
    "v_pk_fma_f32 v[56:57], v[56:57], s[34:35], v[68:69] op_sel_hi:[1,1,0] neg_lo:[0,0,1] neg_hi:[0,0,1]\n\t"
    "v_pk_fma_f32 v[58:59], v[58:59], s[34:35], v[68:69] op_sel_hi:[1,1,0] neg_lo:[0,0,1] neg_hi:[0,0,1]\n\t"
    "v_pk_fma_f32 v[60:61], v[60:61], s[34:35], v[68:69] op_sel_hi:[1,1,0] neg_lo:[0,0,1] neg_hi:[0,0,1]\n\t"
    "v_pk_fma_f32 v[62:63], v[62:63], s[34:35], v[68:69] op_sel_hi:[1,1,0] neg_lo:[0,0,1] neg_hi:[0,0,1]\n\t"
    "v_exp_f32 v56, v56\n\t"
    "v_exp_f32 v57, v57\n\t"
    "v_exp_f32 v58, v58\n\t"
    "v_exp_f32 v59, v59\n\t"
    "v_exp_f32 v60, v60\n\t"
    "v_exp_f32 v61, v61\n\t"
    "v_exp_f32 v62, v62\n\t"
    "v_exp_f32 v63, v63\n\t"
    "v_pk_fma_f32 v[64:65], v[64:65], s[34:35], v[68:69] op_sel_hi:[1,1,0] neg_lo:[0,0,1] neg_hi:[0,0,1]\n\t"
    "v_pk_fma_f32 v[66:67], v[66:67], s[34:35], v[68:69] op_sel_hi:[1,1,0] neg_lo:[0,0,1] neg_hi:[0,0,1]\n\t"
    "v_cvt_pk_bf16_f32 v72, v56, v57\n\t"
    "v_cvt_pk_bf16_f32 v73, v58, v59\n\t"
    "v_cvt_pk_bf16_f32 v74, v60, v61\n\t"
    "v_cvt_pk_bf16_f32 v75, v62, v63\n\t"
    "v_exp_f32 v64, v64\n\t"
    "v_exp_f32 v65, v65\n\t"
    "v_mfma_f32_16x16x32_bf16 v[56:59], v[16:19], v[72:75], 0\n\t"
    "v_mfma_f32_16x16x32_bf16 v[60:63], v[20:23], v[72:75], 0\n\t"
    "v_exp_f32 v66, v66\n\t"
    "v_exp_f32 v67, v67\n\t"
    "v_mfma_f32_16x16x32_bf16 v[68:71], v[84:87], v[72:75], 0\n\t"
    "v_cvt_pk_bf16_f32 v76, v64, v65\n\t"
    "v_cvt_pk_bf16_f32 v77, v66, v67\n\t"
    "v_cvt_pk_bf16_f32 v78, v120, v121\n\t"
    "v_cvt_pk_bf16_f32 v79, v122, v123\n\t"
    "v_cvt_pk_bf16_f32 v80, v124, v125\n\t"
    "v_cvt_pk_bf16_f32 v81, v126, v127\n\t"
    "v_mfma_f32_16x16x16_bf16 v[64:67], v[84:85], v[76:77], 0\n\t"
    "v_mfma_f32_16x16x16_bf16 v[56:59], v[78:79], v[76:77], v[56:59]\n\t"
    "v_mfma_f32_16x16x16_bf16 v[60:63], v[80:81], v[76:77], v[60:63]\n\t"
    "v_mov_b64 v[72:73], 0\n\t"
    "v_mov_b64 v[74:75], 0\n\t"
    "v_mov_b64 v[76:77], 0\n\t"
    "v_mov_b64 v[78:79], 0\n\t"
    "v_mov_b64 v[80:81], 0\n\t"
    "v_mov_b64 v[82:83], 0\n\t"
    "v_fmac_f32 v68, 0.5, v64\n\t"
    "v_rcp_f32 v68, v68\n\t"
    "v_mov_b64 v[84:85], 0\n\t"
    "v_mov_b64 v[86:87], 0\n\t"
    "v_pk_mul_f32 v[56:57], v[56:57], v[68:69] op_sel_hi:[1,0]\n\t"
    "v_pk_mul_f32 v[60:61], v[60:61], v[68:69] op_sel_hi:[1,0]\n\t"
    "v_pk_mul_f32 v[58:59], v[58:59], v[68:69] op_sel_hi:[1,0]\n\t"
    "v_pk_mul_f32 v[62:63], v[62:63], v[68:69] op_sel_hi:[1,0]\n\t"
    "v_cvt_pk_bf16_f32 v64, v56, v60\n\t"
    "v_cvt_pk_bf16_f32 v65, v57, v61\n\t"
    "v_cvt_pk_bf16_f32 v66, v58, v62\n\t"
    "v_cvt_pk_bf16_f32 v67, v59, v63\n\t"
    "s_mov_b64 exec, s[58:59]\n\t"
    "ds_write_b128 v6, v[64:67]\n\t"
    "s_mov_b64 exec, -1\n\t"
    "s_cmp_ge_i32 s64, s66\n\t"
    "s_cbranch_scc0 HGW%=\n\t"
    "HGWd%=:\n\t"
    "s_mov_b32 m0, s68\n\t"
    "s_nop 0\n\t"
    "buffer_load_dwordx4 v1, s[36:39], s48 offen sc1 lds\n\t"
    "buffer_load_dwordx4 v1, s[36:39], s48 offen offset:1024 sc1 lds\n\t"
    "s_mov_b32 m0, s69\n\t"
    "s_nop 0\n\t"
    "buffer_load_dwordx4 v1, s[36:39], s49 offen sc1 lds\n\t"
    "buffer_load_dwordx4 v1, s[36:39], s49 offen offset:1024 sc1 lds\n\t"
    "v_mov_b64 v[56:57], 0\n\t"
    "v_mov_b64 v[58:59], 0\n\t"
    "v_mov_b64 v[60:61], 0\n\t"
    "v_mov_b64 v[62:63], 0\n\t"
    "v_mov_b64 v[68:69], 0\n\t"
    "v_mov_b64 v[70:71], 0\n\t"
    "s_waitcnt lgkmcnt(0)\n\t"
    "s_barrier\n\t"
    "ds_read_b128 v[16:19], v7\n\t"
    "ds_read_b128 v[20:23], v7 offset:128\n\t"
    "v_mov_b64 v[64:65], 0\n\t"
    "v_mov_b64 v[66:67], 0\n\t"
    "s_waitcnt lgkmcnt(1)\n\t"
    "v_smfmac_f32_16x16x64_bf16 v[56:59], v[16:19], v[128:135], v11\n\t"
    "v_smfmac_f32_16x16x64_bf16 v[64:67], v[16:19], v[144:151], v11\n\t"
    "v_smfmac_f32_16x16x64_bf16 v[60:63], v[16:19], v[136:143], v11\n\t"
    "v_smfmac_f32_16x16x64_bf16 v[68:71], v[16:19], v[152:159], v11\n\t"
    "ds_read_b128 v[32:35], v3 offset:32768\n\t"
    "ds_read_b128 v[36:39], v3 offset:33792\n\t"
    "ds_read_b128 v[96:99], v3 offset:49152\n\t"
    "ds_read_b128 v[100:103], v3 offset:50176\n\t"
    "ds_read_b128 v[48:51], v3 offset:40960\n\t"
    "ds_read_b128 v[52:55], v3 offset:41984\n\t"
    "ds_read_b128 v[112:115], v3 offset:57344\n\t"
    "ds_read_b128 v[116:119], v3 offset:58368\n\t"
    "s_waitcnt lgkmcnt(8)\n\t"
    "v_smfmac_f32_16x16x64_bf16 v[56:59], v[20:23], v[24:31], v11\n\t"
    "v_smfmac_f32_16x16x64_bf16 v[64:67], v[20:23], v[88:95], v11\n\t"
    "v_smfmac_f32_16x16x64_bf16 v[60:63], v[20:23], v[40:47], v11\n\t"
    "v_smfmac_f32_16x16x64_bf16 v[68:71], v[20:23], v[104:111], v11\n\t"
    "v_smfmac_f32_16x16x64_bf16 v[72:75], v[16:19], v[160:167], v11\n\t"
    "v_smfmac_f32_16x16x64_bf16 v[80:83], v[16:19], v[176:183], v11\n\t"
    "v_smfmac_f32_16x16x64_bf16 v[76:79], v[16:19], v[168:175], v11\n\t"
    "v_smfmac_f32_16x16x64_bf16 v[84:87], v[16:19], v[184:191], v11\n\t"
    "s_nop 1\n\t"
    "v_permlane32_swap_b32 v56, v64\n\t"
    "v_permlane32_swap_b32 v57, v65\n\t"
    "v_permlane32_swap_b32 v58, v66\n\t"
    "s_waitcnt lgkmcnt(6)\n\t"
    "v_smfmac_f32_16x16x64_bf16 v[72:75], v[20:23], v[32:39], v11\n\t"
    "v_permlane32_swap_b32 v59, v67\n\t"
    "v_pk_add_f32 v[56:57], v[56:57], v[64:65]\n\t"
    "v_pk_add_f32 v[58:59], v[58:59], v[66:67]\n\t"
    "s_waitcnt lgkmcnt(4)\n\t"
    "v_smfmac_f32_16x16x64_bf16 v[80:83], v[20:23], v[96:103], v11\n\t"
    "v_permlane32_swap_b32 v60, v68\n\t"
    "v_permlane32_swap_b32 v61, v69\n\t"
    "v_permlane32_swap_b32 v62, v70\n\t"
    "s_waitcnt lgkmcnt(2)\n\t"
    "v_smfmac_f32_16x16x64_bf16 v[76:79], v[20:23], v[48:55], v11\n\t"
    "v_permlane32_swap_b32 v63, v71\n\t"
    "v_pk_add_f32 v[60:61], v[60:61], v[68:69]\n\t"
    "v_pk_add_f32 v[62:63], v[62:63], v[70:71]\n\t"
    "s_waitcnt lgkmcnt(0)\n\t"
    "v_smfmac_f32_16x16x64_bf16 v[84:87], v[20:23], v[112:119], v11\n\t"
    "v_cvt_pk_bf16_f32 v64, v56, v60\n\t"
    "v_cvt_pk_bf16_f32 v65, v57, v61\n\t"
    "v_cvt_pk_bf16_f32 v66, v58, v62\n\t"
    "v_cvt_pk_bf16_f32 v67, v59, v63\n\t"
    "buffer_store_dwordx4 v[64:67], v8, s[36:39], s46 offen sc1\n\t"
    "s_add_u32 s47, s46, 65536\n\t"
    "v_permlane32_swap_b32 v72, v80\n\t"
    "v_permlane32_swap_b32 v73, v81\n\t"
    "v_permlane32_swap_b32 v74, v82\n\t"
    "v_permlane32_swap_b32 v75, v83\n\t"
    "v_pk_add_f32 v[72:73], v[72:73], v[80:81]\n\t"
    "v_pk_add_f32 v[74:75], v[74:75], v[82:83]\n\t"
    "v_permlane32_swap_b32 v76, v84\n\t"
    "v_permlane32_swap_b32 v77, v85\n\t"
    "v_permlane32_swap_b32 v78, v86\n\t"
    "v_permlane32_swap_b32 v79, v87\n\t"
    "v_pk_add_f32 v[76:77], v[76:77], v[84:85]\n\t"
    "v_pk_add_f32 v[78:79], v[78:79], v[86:87]\n\t"
    "v_cvt_pk_bf16_f32 v80, v72, v76\n\t"
    "v_cvt_pk_bf16_f32 v81, v73, v77\n\t"
    "v_cvt_pk_bf16_f32 v82, v74, v78\n\t"
    "v_cvt_pk_bf16_f32 v83, v75, v79\n\t"
    "buffer_store_dwordx4 v[80:83], v8, s[36:39], s47 offen sc1\n\t"
    "ds_read_b128 v[56:59], v13\n\t"
    "v_permlane32_swap_b32 v120, v124\n\t"
    "v_permlane32_swap_b32 v121, v125\n\t"
    "v_permlane32_swap_b32 v122, v126\n\t"
    "v_permlane32_swap_b32 v123, v127\n\t"
    "v_add_f32 v120, v120, v124\n\t"
    "v_add_f32 v121, v121, v125\n\t"
    "v_add_f32 v122, v122, v126\n\t"
    "v_add_f32 v123, v123, v127\n\t"
    "v_cvt_pk_bf16_f32 v60, v120, v121\n\t"
    "v_cvt_pk_bf16_f32 v61, v122, v123\n\t"
    "buffer_store_dwordx2 v[60:61], v14, s[36:39], s50 offen sc1\n\t"
    "s_waitcnt lgkmcnt(0)\n\t"
    "s_mov_b64 exec, s[58:59]\n\t"
    "buffer_store_dwordx4 v[56:59], v12, s[36:39], s50 offen sc1\n\t"
    "s_mov_b64 exec, -1\n\t"
    "s_add_u32 s50, s50, 131072\n\t"
    "s_xor_b32 s46, s46, 524288\n\t"
    "s_add_u32 s44, s44, 1\n\t"
    "s_cmp_ge_u32 s44, 64\n\t"
    "s_cbranch_scc1 HEND%=\n\t"
    "s_add_u32 s66, s44, 1\n\t"
    "s_min_u32 s66, s66, 63\n\t"
    "s_cmp_lt_u32 s44, 63\n\t"
    "s_cselect_b32 s55, 262144, 0\n\t"
    "s_add_u32 s48, s48, s55\n\t"
    "s_add_u32 s49, s49, s55\n\t"
    "v_mov_b64 v[88:89], 0\n\t"
    "v_mov_b64 v[90:91], 0\n\t"
    "v_mov_b64 v[92:93], 0\n\t"
    "v_mov_b64 v[94:95], 0\n\t"
    "v_mov_b64 v[96:97], 0\n\t"
    "v_mov_b64 v[98:99], 0\n\t"
    "v_mov_b64 v[100:101], 0\n\t"
    "v_mov_b64 v[102:103], 0\n\t"
    "v_mov_b64 v[120:121], 0\n\t"
    "v_mov_b64 v[122:123], 0\n\t"
    "v_mov_b64 v[124:125], 0\n\t"
    "v_mov_b64 v[126:127], 0\n\t"
    "s_waitcnt vmcnt(4)\n\t"
    "s_barrier\n\t"
    "v_add_u32 v65, 147456, v0\n\t"
    "ds_read_b128 v[24:27], v65 offset:0\n\t"
    "ds_read_b128 v[28:31], v65 offset:1024\n\t"
    "ds_read_b128 v[32:35], v65 offset:2048\n\t"
    "ds_read_b128 v[36:39], v65 offset:3072\n\t"
    "ds_read_b128 v[40:43], v65 offset:4096\n\t"
    "ds_read_b128 v[44:47], v65 offset:5120\n\t"
    "ds_read_b128 v[48:51], v65 offset:6144\n\t"
    "ds_read_b128 v[52:55], v65 offset:7168\n\t"
    "ds_read_b128 v[16:19], v10 offset:0\n\t"
    "ds_read_b128 v[20:23], v10 offset:64\n\t"
    "s_cmp_eq_u32 s51, 0\n\t"
    "s_cbranch_scc1 HPRE0_%=\n\t"
    "s_cmp_eq_u32 s51, 1\n\t"
    "s_cbranch_scc1 HPRE1_%=\n\t"
    "s_cmp_eq_u32 s51, 2\n\t"
    "s_cbranch_scc1 HPRE2_%=\n\t"
    "s_branch HPRE3_%=\n\t"
    "HPRE0_%=:\n\t"
    "ds_read_b128 v[104:107], v2 offset:0\n\t"
    "ds_read_b128 v[108:111], v2 offset:1024\n\t"
    "ds_read_b128 v[112:115], v2 offset:8192\n\t"
    "ds_read_b128 v[116:119], v2 offset:9216\n\t"
    "s_waitcnt lgkmcnt(0)\n\t"
    "s_branch HSTEP0_%=\n\t"
    "HPRE1_%=:\n\t"
    "ds_read_b128 v[104:107], v2 offset:16384\n\t"
    "ds_read_b128 v[108:111], v2 offset:17408\n\t"
    "ds_read_b128 v[112:115], v2 offset:24576\n\t"
    "ds_read_b128 v[116:119], v2 offset:25600\n\t"
    "s_waitcnt lgkmcnt(0)\n\t"
    "s_branch HSTEP1_%=\n\t"
    "HPRE2_%=:\n\t"
    "ds_read_b128 v[104:107], v2 offset:32768\n\t"
    "ds_read_b128 v[108:111], v2 offset:33792\n\t"
    "ds_read_b128 v[112:115], v2 offset:40960\n\t"
    "ds_read_b128 v[116:119], v2 offset:41984\n\t"
    "s_waitcnt lgkmcnt(0)\n\t"
    "s_branch HSTEP2_%=\n\t"
    "HPRE3_%=:\n\t"
    "ds_read_b128 v[104:107], v2 offset:49152\n\t"
    "ds_read_b128 v[108:111], v2 offset:50176\n\t"
    "ds_read_b128 v[112:115], v2 offset:57344\n\t"
    "ds_read_b128 v[116:119], v2 offset:58368\n\t"
    "s_waitcnt lgkmcnt(0)\n\t"
    "s_branch HSTEP3_%=\n\t"
    "HLAST0_%=:\n\t"
    "v_smfmac_f32_16x16x64_bf16 v[120:123], v[76:79], v[208:215], v11\n\t"
    "v_smfmac_f32_16x16x64_bf16 v[124:127], v[76:79], v[216:223], v11\n\t"
    "v_smfmac_f32_16x16x64_bf16 v[120:123], v[80:83], v[224:231], v11\n\t"
    "v_smfmac_f32_16x16x64_bf16 v[124:127], v[80:83], v[232:239], v11\n\t"
    "v_smfmac_f32_16x16x64_bf16 v[120:123], v[84:87], v[240:247], v11\n\t"
    "v_smfmac_f32_16x16x64_bf16 v[124:127], v[84:87], v[248:255], v11\n\t"
    "s_branch HLASTC%=\n\t"
    "HLAST1_%=:\n\t"
    "v_smfmac_f32_16x16x64_bf16 v[120:123], v[84:87], v[240:247], v11\n\t"
    "v_smfmac_f32_16x16x64_bf16 v[124:127], v[84:87], v[248:255], v11\n\t"
    "v_smfmac_f32_16x16x64_bf16 v[120:123], v[56:59], a[192:199], v11\n\t"
    "v_smfmac_f32_16x16x64_bf16 v[124:127], v[56:59], a[200:207], v11\n\t"
    "v_smfmac_f32_16x16x64_bf16 v[120:123], v[60:63], a[208:215], v11\n\t"
    "v_smfmac_f32_16x16x64_bf16 v[124:127], v[60:63], a[216:223], v11\n\t"
    "s_branch HLASTC%=\n\t"
    "HLAST2_%=:\n\t"
    "v_smfmac_f32_16x16x64_bf16 v[120:123], v[60:63], a[208:215], v11\n\t"
    "v_smfmac_f32_16x16x64_bf16 v[124:127], v[60:63], a[216:223], v11\n\t"
    "v_smfmac_f32_16x16x64_bf16 v[120:123], v[64:67], a[224:231], v11\n\t"
    "v_smfmac_f32_16x16x64_bf16 v[124:127], v[64:67], a[232:239], v11\n\t"
    "v_smfmac_f32_16x16x64_bf16 v[120:123], v[68:71], a[240:247], v11\n\t"
    "v_smfmac_f32_16x16x64_bf16 v[124:127], v[68:71], a[248:255], v11\n\t"
    "s_branch HLASTC%=\n\t"
    "HLAST3_%=:\n\t"
    "v_smfmac_f32_16x16x64_bf16 v[120:123], v[68:71], a[240:247], v11\n\t"
    "v_smfmac_f32_16x16x64_bf16 v[124:127], v[68:71], a[248:255], v11\n\t"
    "v_smfmac_f32_16x16x64_bf16 v[120:123], v[72:75], v[192:199], v11\n\t"
    "v_smfmac_f32_16x16x64_bf16 v[124:127], v[72:75], v[200:207], v11\n\t"
    "v_smfmac_f32_16x16x64_bf16 v[120:123], v[76:79], v[208:215], v11\n\t"
    "v_smfmac_f32_16x16x64_bf16 v[124:127], v[76:79], v[216:223], v11\n\t"
    "HLASTC%=:\n\t"
    "ds_read_b128 v[56:59], v13\n\t"
    "s_nop 7\n\t"
    "v_permlane32_swap_b32 v120, v124\n\t"
    "v_permlane32_swap_b32 v121, v125\n\t"
    "v_permlane32_swap_b32 v122, v126\n\t"
    "v_permlane32_swap_b32 v123, v127\n\t"
    "v_add_f32 v120, v120, v124\n\t"
    "v_add_f32 v121, v121, v125\n\t"
    "v_add_f32 v122, v122, v126\n\t"
    "v_add_f32 v123, v123, v127\n\t"
    "v_cvt_pk_bf16_f32 v60, v120, v121\n\t"
    "v_cvt_pk_bf16_f32 v61, v122, v123\n\t"
    "buffer_store_dwordx2 v[60:61], v14, s[36:39], s50 offen sc1\n\t"
    "s_waitcnt lgkmcnt(0)\n\t"
    "s_mov_b64 exec, s[58:59]\n\t"
    "buffer_store_dwordx4 v[56:59], v12, s[36:39], s50 offen sc1\n\t"
    "s_mov_b64 exec, -1\n\t"
    "HEND%=:\n\t"
    "s_waitcnt vmcnt(0)\n\t"
    "v_mov_b32 v63, 63\n\t"
    "buffer_store_dword v63, off, s[36:39], s63 sc1\n\t"
    "s_branch HDONE%=\n\t"
    "HGW%=:\n\t"
    "s_mov_b32 s54, 1000000\n\t"
    "HGW1%=:\n\t"
    "buffer_load_dword v71, v15, s[36:39], 0 offen sc1\n\t"
    "s_waitcnt vmcnt(0)\n\t"
    "v_readlane_b32 s55, v71, 0\n\t"
    "v_readlane_b32 s57, v71, 1\n\t"
    "s_min_i32 s64, s55, s57\n\t"
    "s_cmp_ge_i32 s64, s66\n\t"
    "s_nop 0\n\t"
    "s_cbranch_scc1 HGWd%=\n\t"
    "s_sub_u32 s54, s54, 1\n\t"
    "s_cbranch_scc1 HFAIL%=\n\t"
    "s_sleep 1\n\t"
    "s_branch HGW1%=\n\t"
    "HFAIL%=:\n\t"
    "s_mov_b32 s55, 2117760\n\t"
    "v_mov_b32 v63, 2\n\t"
    "buffer_store_dword v63, off, s[36:39], s55 sc1\n\t"
    "HDONE%=:\n\t"
    "s_waitcnt vmcnt(0) lgkmcnt(0)\n\t"
    :
    : [ws_lo] "s"((unsigned)ws), [ws_hi] "s"((unsigned)(ws >> 32)), [wq_lo] "s"((unsigned)wq), [wq_hi] "s"((unsigned)(wq >> 32)),
      [wk_lo] "s"((unsigned)wk), [wk_hi] "s"((unsigned)(wk >> 32)), [wv_lo] "s"((unsigned)wv), [wv_hi] "s"((unsigned)(wv >> 32)),
      [wo_lo] "s"((unsigned)wo), [wo_hi] "s"((unsigned)(wo >> 32)), [m0_lo] "s"((unsigned)m0), [m0_hi] "s"((unsigned)(m0 >> 32)),
      [h] "s"(h), [w] "s"(w)
    : "memory", "scc", "vcc", "s29", "s30", "s31", "s32", "s33", "s34", "s35", "s36", "s37", "s38", "s39", "s40", "s41", "s42", "s43", "s44", "s45", "s46", "s47", "s48", "s49", "s50", "s51", "s52", "s53", "s54", "s55", "s56", "s57", "s58", "s59", "s60", "s61", "s62", "s63", "s64", "s65", "s66", "s67", "s68", "s69", "s70", "s71", "v0", "v1", "v2", "v3", "v4", "v5", "v6", "v7", "v8", "v9", "v10", "v11", "v12", "v13", "v14", "v15", "v16", "v17", "v18", "v19", "v20", "v21", "v22", "v23", "v24", "v25", "v26", "v27", "v28", "v29", "v30", "v31", "v32", "v33", "v34", "v35", "v36", "v37", "v38", "v39", "v40", "v41", "v42", "v43", "v44", "v45", "v46", "v47", "v48", "v49", "v50", "v51", "v52", "v53", "v54", "v55", "v56", "v57", "v58", "v59", "v60", "v61", "v62", "v63", "v64", "v65", "v66", "v67", "v68", "v69", "v70", "v71", "v72", "v73", "v74", "v75", "v76", "v77", "v78", "v79", "v80", "v81", "v82", "v83", "v84", "v85", "v86", "v87", "v88", "v89", "v90", "v91", "v92", "v93", "v94", "v95", "v96", "v97", "v98", "v99", "v100", "v101", "v102", "v103", "v104", "v105", "v106", "v107", "v108", "v109", "v110", "v111", "v112", "v113", "v114", "v115", "v116", "v117", "v118", "v119", "v120", "v121", "v122", "v123", "v124", "v125", "v126", "v127", "v128", "v129", "v130", "v131", "v132", "v133", "v134", "v135", "v136", "v137", "v138", "v139", "v140", "v141", "v142", "v143", "v144", "v145", "v146", "v147", "v148", "v149", "v150", "v151", "v152", "v153", "v154", "v155", "v156", "v157", "v158", "v159", "v160", "v161", "v162", "v163", "v164", "v165", "v166", "v167", "v168", "v169", "v170", "v171", "v172", "v173", "v174", "v175", "v176", "v177", "v178", "v179", "v180", "v181", "v182", "v183", "v184", "v185", "v186", "v187", "v188", "v189", "v190", "v191", "v192", "v193", "v194", "v195", "v196", "v197", "v198", "v199", "v200", "v201", "v202", "v203", "v204", "v205", "v206", "v207", "v208", "v209", "v210", "v211", "v212", "v213", "v214", "v215", "v216", "v217", "v218", "v219", "v220", "v221", "v222", "v223", "v224", "v225", "v226", "v227", "v228", "v229", "v230", "v231", "v232", "v233", "v234", "v235", "v236", "v237", "v238", "v239", "v240", "v241", "v242", "v243", "v244", "v245", "v246", "v247", "v248", "v249", "v250", "v251", "v252", "v253", "v254", "v255", "a0", "a1", "a2", "a3", "a4", "a5", "a6", "a7", "a8", "a9", "a10", "a11", "a12", "a13", "a14", "a15", "a16", "a17", "a18", "a19", "a20", "a21", "a22", "a23", "a24", "a25", "a26", "a27", "a28", "a29", "a30", "a31", "a32", "a33", "a34", "a35", "a36", "a37", "a38", "a39", "a40", "a41", "a42", "a43", "a44", "a45", "a46", "a47", "a48", "a49", "a50", "a51", "a52", "a53", "a54", "a55", "a56", "a57", "a58", "a59", "a60", "a61", "a62", "a63", "a64", "a65", "a66", "a67", "a68", "a69", "a70", "a71", "a72", "a73", "a74", "a75", "a76", "a77", "a78", "a79", "a80", "a81", "a82", "a83", "a84", "a85", "a86", "a87", "a88", "a89", "a90", "a91", "a92", "a93", "a94", "a95", "a96", "a97", "a98", "a99", "a100", "a101", "a102", "a103", "a104", "a105", "a106", "a107", "a108", "a109", "a110", "a111", "a112", "a113", "a114", "a115", "a116", "a117", "a118", "a119", "a120", "a121", "a122", "a123", "a124", "a125", "a126", "a127", "a128", "a129", "a130", "a131", "a132", "a133", "a134", "a135", "a136", "a137", "a138", "a139", "a140", "a141", "a142", "a143", "a144", "a145", "a146", "a147", "a148", "a149", "a150", "a151", "a152", "a153", "a154", "a155", "a156", "a157", "a158", "a159", "a160", "a161", "a162", "a163", "a164", "a165", "a166", "a167", "a168", "a169", "a170", "a171", "a172", "a173", "a174", "a175", "a176", "a177", "a178", "a179", "a180", "a181", "a182", "a183", "a184", "a185", "a186", "a187", "a188", "a189", "a190", "a191", "a192", "a193", "a194", "a195", "a196", "a197", "a198", "a199", "a200", "a201", "a202", "a203", "a204", "a205", "a206", "a207", "a208", "a209", "a210", "a211", "a212", "a213", "a214", "a215", "a216", "a217", "a218", "a219", "a220", "a221", "a222", "a223", "a224", "a225", "a226", "a227", "a228", "a229", "a230", "a231", "a232", "a233", "a234", "a235", "a236", "a237", "a238", "a239", "a240", "a241", "a242", "a243", "a244", "a245", "a246", "a247", "a248", "a249", "a250", "a251", "a252", "a253", "a254", "a255"
  );
}

__device__ __forceinline__ unsigned calibrate(const Params& p) {
  unsigned best[2] = {~0u, ~0u};
#pragma unroll
  for (int it = 0; it < 2 * NPROBE; it++) {
    const int k = it & 1;
    GAS unsigned* a = (GAS unsigned*)(p.ws + OFF_CTL + k * 8192 + (it >> 1) * 256);
    unsigned long long t0, t1; unsigned v;
    asm volatile("s_memtime %0\n\ts_waitcnt lgkmcnt(0)" : "=s"(t0) :: "memory");
    v = it;
    asm volatile("global_store_dword %0, %1, off sc1\n\ts_waitcnt vmcnt(0)" :: "v"(a), "v"(v) : "memory");
    asm volatile("s_memtime %0\n\ts_waitcnt lgkmcnt(0)" : "=s"(t1) :: "memory");
    best[k] = min(best[k], (unsigned)(t1 - t0));
  }
  return best[1] < best[0] ? 1 : 0;
}

__device__ __forceinline__ void red_role(const Params& p, int j) {
  const int w = threadIdx.x >> 6, l = threadIdx.x & 63;
  unsigned mysel = 0;
  if (j == CALJ && w == CALW) mysel = calibrate(p);
  const unsigned epoch = (unsigned)ld_flag(p.ws, flg(FLG_EPOCH)) + 1;
  if (j == CALJ && w == CALW && l == 0) st_tag(p.ws, flg(FLG_SEL), epoch, mysel);
  const auto rs = __builtin_amdgcn_make_buffer_rsrc(p.ws, 0, WS_USED, 0x00020000);
  {
    u4 v[8];
#pragma unroll
    for (int i = 0; i < 8; i++) v[i] = *(const u4*)(p.x + (size_t)(j * 32 + w * 8 + i) * 512 + l * 8);
#pragma unroll
    for (int i = 0; i < 8; i++) {
      float f[8];
#pragma unroll
      for (int k = 0; k < 4; k++) { f[2 * k] = b2f(v[i][k] & 0xffffu); f[2 * k + 1] = __uint_as_float(v[i][k] & 0xffff0000u); }
      const float mean = wave_sum(((f[0] + f[1]) + (f[2] + f[3])) + ((f[4] + f[5]) + (f[6] + f[7]))) * (1.f / 512.f);
      float q = 0;
#pragma unroll
      for (int k = 0; k < 8; k++) q += (f[k] - mean) * (f[k] - mean);
      q = wave_sum(q);
      const float m = rbf(mean), rden = 1.f / rbf(sqrtf(rbf(rbf(q * (1.f / 512.f)) + 1e-5f)));
      u4 o;
#pragma unroll
      for (int k = 0; k < 4; k++) o[k] = pk2(rbf(f[2 * k] - m) * rden, rbf(f[2 * k + 1] - m) * rden);
      __builtin_amdgcn_raw_buffer_store_b128(o, rs, OFF_XN + (j * 32 + w * 8 + i) * 1024 + l * 16, 0, 16);
    }
    WAIT_VM(0);
    if (l == 0) st_tag(p.ws, flg(FLG_LN + j) + w * 8, epoch, 1);
  }
  if (w != 0) return;
  {
    const u4 s = {~0u, ~0u, ~0u, ~0u};
#pragma unroll 1
    for (int k = 0; k < 16; k++)
      if (k >= 2 || l >= 8) __builtin_amdgcn_raw_buffer_store_b128(s, rs, OFF_CTL + (8 * (k >> 1) + (l >> 3)) * 16384 + (k & 1) * 8192 + j * 128 + (l & 7) * 16, 0, 16);
#pragma unroll 1
    for (int k = 0; k < 16; k++)
      __builtin_amdgcn_raw_buffer_store_b128(s, rs, OFF_PRING + (k >> 3) * 524288 + (j >> 1) * 16384 + ((k >> 2) & 1) * 8192 + (j & 1) * 4096 + (k & 3) * 1024 + l * 16, 0, 16);
    WAIT_VM(0);
    if (l == 0) st_tag(p.ws, flg(INIT_R + j), epoch, 1);
  }
  unsigned sel;
  if (!wait_tags(p.ws, flg(FLG_SEL), true, epoch, &sel)) return;
  sel = __builtin_amdgcn_readfirstlane(sel);
  const uint64_t ws = (uint64_t)p.ws;
  asm volatile(
    "s_mov_b32 s36, %[ws_lo]\n\t"
    "s_and_b32 s37, %[ws_hi], 0xffff\n\t"
    "s_mov_b32 s38, 214958080\n\t"
    "s_mov_b32 s39, 0x20000\n\t"
    "v_mbcnt_lo_u32_b32 v0, -1, 0\n\t"
    "v_mbcnt_hi_u32_b32 v0, -1, v0\n\t"
    "s_lshr_b32 s55, %[j], 1\n\t"
    "s_lshl_b32 s55, s55, 14\n\t"
    "s_and_b32 s56, %[j], 1\n\t"
    "s_lshl_b32 s56, s56, 12\n\t"
    "s_add_u32 s55, s55, s56\n\t"
    "v_lshlrev_b32 v1, 4, v0\n\t"
    "v_add_u32 v1, s55, v1\n\t"
    "v_bfe_u32 v3, v0, 2, 1\n\t"
    "v_lshlrev_b32 v2, 2, v3\n\t"
    "v_bfe_u32 v3, v0, 4, 1\n\t"
    "v_lshl_add_u32 v2, v3, 1, v2\n\t"
    "v_bfe_u32 v3, v0, 3, 1\n\t"
    "v_add_u32 v2, v2, v3\n\t"
    "v_lshlrev_b32 v2, 4, v2\n\t"
    "v_and_b32 v3, 3, v0\n\t"
    "v_lshl_add_u32 v2, v3, 2, v2\n\t"
    "s_lshl_b32 s55, %[j], 7\n\t"
    "v_add_u32 v2, s55, v2\n\t"
    "v_and_b32 v3, 15, v0\n\t"
    "v_lshrrev_b32 v8, 1, v3\n\t"
    "v_and_b32 v9, 1, v3\n\t"
    "v_lshlrev_b32 v9, 4, v9\n\t"
    "v_mov_b32 v10, 0x3f80\n\t"
    "v_lshlrev_b32 v10, v9, v10\n\t"
    "v_cmp_eq_u32 vcc, 0, v8\n\t"
    "s_nop 1\n\t"
    "v_cndmask_b32 v4, 0, v10, vcc\n\t"
    "v_cmp_eq_u32 vcc, 1, v8\n\t"
    "s_nop 1\n\t"
    "v_cndmask_b32 v5, 0, v10, vcc\n\t"
    "v_cmp_eq_u32 vcc, 2, v8\n\t"
    "s_nop 1\n\t"
    "v_cndmask_b32 v6, 0, v10, vcc\n\t"
    "v_cmp_eq_u32 vcc, 3, v8\n\t"
    "s_nop 1\n\t"
    "v_cndmask_b32 v7, 0, v10, vcc\n\t"
    "v_cmp_gt_u32 vcc, 8, v3\n\t"
    "s_nop 1\n\t"
    "v_cndmask_b32 v4, 0, v4, vcc\n\t"
    "v_cndmask_b32 v5, 0, v5, vcc\n\t"
    "v_cndmask_b32 v6, 0, v6, vcc\n\t"
    "v_cndmask_b32 v7, 0, v7, vcc\n\t"
    "s_mov_b32 s58, 0xff00ff00\n\t"
    "s_mov_b32 s59, 0xff00ff00\n\t"
    "v_mov_b32 v12, -1\n\t"
    "v_mov_b32 v13, -1\n\t"
    "v_mov_b32 v14, -1\n\t"
    "v_mov_b32 v15, -1\n\t"
    "s_lshl_b32 s53, %[sel], 13\n\t"
    "s_add_u32 s45, s53, 16384\n\t"
    "s_add_u32 s46, s53, 1048576\n\t"
    "s_mov_b32 s44, 0\n\t"
    "RSTEP%=:\n\t"
    "s_mov_b32 s54, 2000000\n\t"
    "buffer_load_dwordx4 v[20:23], v1, s[36:39], s46 offen offset:0 sc1\n\t"
    "buffer_load_dwordx4 v[24:27], v1, s[36:39], s46 offen offset:1024 sc1\n\t"
    "buffer_load_dwordx4 v[28:31], v1, s[36:39], s46 offen offset:2048 sc1\n\t"
    "buffer_load_dwordx4 v[32:35], v1, s[36:39], s46 offen offset:3072 sc1\n\t"
    "buffer_load_dwordx4 v[36:39], v1, s[36:39], s46 offen offset:0 sc1\n\t"
    "buffer_load_dwordx4 v[40:43], v1, s[36:39], s46 offen offset:1024 sc1\n\t"
    "buffer_load_dwordx4 v[44:47], v1, s[36:39], s46 offen offset:2048 sc1\n\t"
    "buffer_load_dwordx4 v[48:51], v1, s[36:39], s46 offen offset:3072 sc1\n\t"
    "RPOLL%=:\n\t"
    "s_waitcnt vmcnt(7)\n\t"
    "v_mfma_f32_16x16x32_bf16 v[16:19], v[4:7], v[20:23], 0\n\t"
    "v_max3_u32 v10, v20, v21, v22\n\t"
    "v_max_u32 v10, v10, v23\n\t"
    "s_waitcnt vmcnt(6)\n\t"
    "v_mfma_f32_16x16x32_bf16 v[16:19], v[4:7], v[24:27], v[16:19]\n\t"
    "v_max3_u32 v10, v10, v24, v25\n\t"
    "v_max3_u32 v10, v10, v26, v27\n\t"
    "s_waitcnt vmcnt(5)\n\t"
    "v_mfma_f32_16x16x32_bf16 v[16:19], v[4:7], v[28:31], v[16:19]\n\t"
    "v_max3_u32 v10, v10, v28, v29\n\t"
    "v_max3_u32 v10, v10, v30, v31\n\t"
    "s_waitcnt vmcnt(4)\n\t"
    "v_mfma_f32_16x16x32_bf16 v[16:19], v[4:7], v[32:35], v[16:19]\n\t"
    "v_max3_u32 v10, v10, v32, v33\n\t"
    "v_max3_u32 v10, v10, v34, v35\n\t"
    "v_cmp_eq_u32 vcc, -1, v10\n\t"
    "s_nop 3\n\t"
    "s_cbranch_vccz ROK%=\n\t"
    "buffer_load_dwordx4 v[20:23], v1, s[36:39], s46 offen offset:0 sc1\n\t"
    "buffer_load_dwordx4 v[24:27], v1, s[36:39], s46 offen offset:1024 sc1\n\t"
    "buffer_load_dwordx4 v[28:31], v1, s[36:39], s46 offen offset:2048 sc1\n\t"
    "buffer_load_dwordx4 v[32:35], v1, s[36:39], s46 offen offset:3072 sc1\n\t"
    "s_sub_u32 s54, s54, 1\n\t"
    "s_cbranch_scc1 RFAIL%=\n\t"
    "s_waitcnt vmcnt(7)\n\t"
    "v_mfma_f32_16x16x32_bf16 v[16:19], v[4:7], v[36:39], 0\n\t"
    "v_max3_u32 v10, v36, v37, v38\n\t"
    "v_max_u32 v10, v10, v39\n\t"
    "s_waitcnt vmcnt(6)\n\t"
    "v_mfma_f32_16x16x32_bf16 v[16:19], v[4:7], v[40:43], v[16:19]\n\t"
    "v_max3_u32 v10, v10, v40, v41\n\t"
    "v_max3_u32 v10, v10, v42, v43\n\t"
    "s_waitcnt vmcnt(5)\n\t"
    "v_mfma_f32_16x16x32_bf16 v[16:19], v[4:7], v[44:47], v[16:19]\n\t"
    "v_max3_u32 v10, v10, v44, v45\n\t"
    "v_max3_u32 v10, v10, v46, v47\n\t"
    "s_waitcnt vmcnt(4)\n\t"
    "v_mfma_f32_16x16x32_bf16 v[16:19], v[4:7], v[48:51], v[16:19]\n\t"
    "v_max3_u32 v10, v10, v48, v49\n\t"
    "v_max3_u32 v10, v10, v50, v51\n\t"
    "v_cmp_eq_u32 vcc, -1, v10\n\t"
    "s_nop 3\n\t"
    "s_cbranch_vccz ROK%=\n\t"
    "buffer_load_dwordx4 v[36:39], v1, s[36:39], s46 offen offset:0 sc1\n\t"
    "buffer_load_dwordx4 v[40:43], v1, s[36:39], s46 offen offset:1024 sc1\n\t"
    "buffer_load_dwordx4 v[44:47], v1, s[36:39], s46 offen offset:2048 sc1\n\t"
    "buffer_load_dwordx4 v[48:51], v1, s[36:39], s46 offen offset:3072 sc1\n\t"
    "s_sub_u32 s54, s54, 1\n\t"
    "s_cbranch_scc1 RFAIL%=\n\t"
    "s_branch RPOLL%=\n\t"
    "ROK%=:\n\t"
    "v_add_f32_dpp v16, v16, v16 row_ror:8 row_mask:0xf bank_mask:0xf\n\t"
    "v_add_f32_dpp v17, v17, v17 row_ror:8 row_mask:0xf bank_mask:0xf\n\t"
    "v_add_f32_dpp v18, v18, v18 row_ror:8 row_mask:0xf bank_mask:0xf\n\t"
    "v_add_f32_dpp v19, v19, v19 row_ror:8 row_mask:0xf bank_mask:0xf\n\t"
    "v_cvt_pk_bf16_f32 v8, v16, v17\n\t"
    "v_cvt_pk_bf16_f32 v9, v18, v19\n\t"
    "v_cndmask_b32 v8, v8, v9, s[58:59]\n\t"
    "v_min_u32 v8, 0xfffffffe, v8\n\t"
    "s_mov_b32 exec_hi, 0\n\t"
    "buffer_store_dword v8, v2, s[36:39], s45 offen sc1\n\t"
    "s_mov_b64 exec, -1\n\t"
    "s_sleep 2\n\t"
    "s_sleep 2\n\t"
    "s_sleep 2\n\t"
    "s_sleep 2\n\t"
    "buffer_store_dwordx4 v[12:15], v1, s[36:39], s46 offen offset:0 sc1\n\t"
    "buffer_store_dwordx4 v[12:15], v1, s[36:39], s46 offen offset:1024 sc1\n\t"
    "buffer_store_dwordx4 v[12:15], v1, s[36:39], s46 offen offset:2048 sc1\n\t"
    "buffer_store_dwordx4 v[12:15], v1, s[36:39], s46 offen offset:3072 sc1\n\t"
    "s_add_u32 s45, s45, 16384\n\t"
    "s_xor_b32 s46, s46, 524288\n\t"
    "s_add_u32 s44, s44, 1\n\t"
    "s_cmp_lt_u32 s44, 63\n\t"
    "s_cbranch_scc1 RSTEP%=\n\t"
    "s_branch RDONE%=\n\t"
    "RFAIL%=:\n\t"
    "s_mov_b32 s55, 2117760\n\t"
    "v_mov_b32 v8, 1\n\t"
    "buffer_store_dword v8, off, s[36:39], s55 sc1\n\t"
    "RDONE%=:\n\t"
    "s_waitcnt vmcnt(0)\n\t"
    :
    : [ws_lo] "s"((unsigned)ws), [ws_hi] "s"((unsigned)(ws >> 32)), [j] "s"(j), [sel] "s"(sel)
    : "memory", "scc", "vcc", "s36", "s37", "s38", "s39", "s40", "s41", "s42", "s43", "s44", "s45", "s46", "s47", "s48", "s49", "s50", "s51", "s52", "s53", "s54", "s55", "s56", "s57", "s58", "s59", "s60", "s61", "s62", "s63", "s64", "s65", "s66", "s67", "s68", "s69", "s70", "s71", "v0", "v1", "v2", "v3", "v4", "v5", "v6", "v7", "v8", "v9", "v10", "v11", "v12", "v13", "v14", "v15", "v16", "v17", "v18", "v19", "v20", "v21", "v22", "v23", "v24", "v25", "v26", "v27", "v28", "v29", "v30", "v31", "v32", "v33", "v34", "v35", "v36", "v37", "v38", "v39", "v40", "v41", "v42", "v43", "v44", "v45", "v46", "v47", "v48", "v49", "v50", "v51", "v52", "v53", "v54", "v55", "v56", "v57", "v58", "v59", "v60", "v61", "v62", "v63", "v64", "v65", "v66", "v67", "v68", "v69", "v70", "v71", "v72", "v73", "v74", "v75", "v76", "v77", "v78", "v79", "v80", "v81", "v82", "v83", "v84", "v85", "v86", "v87", "v88", "v89", "v90", "v91", "v92", "v93", "v94", "v95", "v96", "v97", "v98", "v99", "v100", "v101", "v102", "v103", "v104", "v105", "v106", "v107", "v108", "v109", "v110", "v111", "v112", "v113", "v114", "v115", "v116", "v117", "v118", "v119", "v120", "v121", "v122", "v123", "v124", "v125", "v126", "v127"
  );
}

__global__ void __launch_bounds__(256, 1) k_main(Params p) {
  extern __shared__ __attribute__((aligned(16))) char smem[];
  const int b = blockIdx.x, xcd = b & 7, idx = b >> 3;
  if (xcd == HX0 || xcd == HX1) {
    if (idx < 16) head_role(p, (xcd == HX0 ? 0 : 16) + idx);
  } else if (xcd < 4) {
    red_role(p, (xcd == RX0 ? 0 : 32) + idx);
  } else {
    const int wi = idx * 4 + (xcd - 4);
    if (wi < 16) g1_role<3>(p, wi, p.wk, nullptr, 4096, OFF_K, flg(FLG_G1 + wi));
    else if (wi < 32) g1_role<1>(p, wi - 16, p.wv, nullptr, 4096, OFF_VT, flg(FLG_G1 + wi));
    else if (wi < 48) g1_role<0>(p, wi - 32, p.wq, nullptr, 4096, OFF_Q, flg(FLG_G1 + wi));
    else if (wi < 62) g1_role<2>(p, wi - 48, p.wg, p.wu, 1792, OFF_H, flg(FLG_G1 + wi));
    else if (wi < 126) t_role(p, (wi - 62) & 31, (wi - 62) >> 5);
  }
  asm volatile("" :: "v"(smem));
}
}

extern "C" void kernel_launch(void* const* d_in, const int* in_sizes, int n_in, void* d_out, int out_size, void* d_ws, size_t ws_size, hipStream_t stream) {
  Params p;
  p.x = (const bf16_t*)d_in[0]; p.mem0 = (const bf16_t*)d_in[1];
  p.wq = (const bf16_t*)d_in[2]; p.wk = (const bf16_t*)d_in[3]; p.wv = (const bf16_t*)d_in[4]; p.wo = (const bf16_t*)d_in[5];
  p.wg = (const bf16_t*)d_in[8]; p.wu = (const bf16_t*)d_in[9]; p.wd = (const bf16_t*)d_in[10];
  p.out = (bf16_t*)d_out;
  p.ws = (char*)(((uintptr_t)d_ws + (2u << 20) - 1) & ~(uintptr_t)((2u << 20) - 1));
  (void)hipFuncSetAttribute((const void*)k_main, hipFuncAttributeMaxDynamicSharedMemorySize, kLds);
  k_main<<<256, 256, kLds, stream>>>(p);
}
```
